```python
import torch

def _softmax(x, axis=-1):
    return torch.softmax(x, dim=axis)
def _gelu(x):
    return torch.nn.functional.gelu(x, approximate="tanh")
def _silu(x):
    return torch.nn.functional.silu(x)
def _sigmoid(x):
    return torch.sigmoid(x)
def _layernorm(x, axis=-1, eps=1e-5):
    m = x.mean(dim=axis, keepdim=True)
    v = x.var(dim=axis, keepdim=True, unbiased=False)
    return (x - m) / torch.sqrt(v + eps)
def _scatter_add(tgt, idx, src, axis=0):
    return tgt.clone().index_add_(axis, idx, src)
def _segment_sum(src, seg, n):
    out = torch.zeros((n,) + src.shape[1:], dtype=src.dtype)
    return out.index_add_(0, seg, src)
def _pad(x, pw):
    flat = tuple(v for pair in reversed(pw) for v in pair)
    return torch.nn.functional.pad(x, flat)

kernel_name = 'sequential_preLN__linear_attn_block__glu_gated_geglu__prompt'

def setup_inputs():

    _w = (lambda a: (lambda t: t.to(torch.bfloat16) if t.is_floating_point() else t)(torch.as_tensor(a)))
    def _A_setup():
        B, S, D = 1, 2048, 1024
        return {
            "x": _w((lambda *s: torch.randn(*s, dtype=torch.bfloat16) * 0.05)(B, S, D)),
            "wqkv": _w((lambda *s: torch.randn(*s, dtype=torch.bfloat16) * 0.05)(D, 3 * D)),
            "wo": _w((lambda *s: torch.randn(*s, dtype=torch.bfloat16) * 0.05)(D, D)),
        }
    _A_in = _A_setup()

    def _B_setup():
        B, S, Dm, Df = 1, 2048, 1024, 3584
        return {
            "x": _w((lambda *s: torch.randn(*s, dtype=torch.bfloat16) * 0.05)(B, S, Dm)),
            "w_gate": _w((lambda *s: torch.randn(*s, dtype=torch.bfloat16) * 0.4)(Dm, Df) * (Dm ** -0.5)),
            "w_up": _w((lambda *s: torch.randn(*s, dtype=torch.bfloat16) * 0.4)(Dm, Df) * (Dm ** -0.5)),
            "w_down": _w((lambda *s: torch.randn(*s, dtype=torch.bfloat16) * 0.4)(Df, Dm) * (Df ** -0.5)),
        }
    _B_in = _B_setup()

    return {
        "A_x": _A_in["x"],
        "A_wqkv": _A_in["wqkv"],
        "A_wo": _A_in["wo"],
        "B_x": _B_in["x"],
        "B_w_gate": _B_in["w_gate"],
        "B_w_up": _B_in["w_up"],
        "B_w_down": _B_in["w_down"],
    }

def reference(A_x, A_wqkv, A_wo, B_x, B_w_gate, B_w_up, B_w_down):
    def _A_ref(x, wqkv, wo):
        B, S, D = x.shape
        H = 32; Dh = D // H
        qkv = (lambda x, *d: x.permute(*d))((x @ wqkv).reshape(B, S, 3, H, Dh), 2, 0, 3, 1, 4)
        q, k, v = qkv[0], qkv[1], qkv[2]
        phi = lambda t: torch.where(t > 0, t + 1, torch.exp(t)) + 1e-6
        pq, pk = phi(q), phi(k)
        kv = torch.einsum("bhsd,bhse->bhde", pk, v)
        z = torch.einsum("bhsd->bhd", pk)
        num = torch.einsum("bhqd,bhde->bhqe", pq, kv)
        den = torch.einsum("bhqd,bhd->bhq", pq, z)[..., None] + 1e-6
        o = num / den
        return (lambda x, *d: x.permute(*d))(o, 0, 2, 1, 3).reshape(B, S, D) @ wo

    def _B_ref(x, w_gate, w_up, w_down):
        g = torch.einsum("bsd,df->bsf", x, w_gate)
        u = torch.einsum("bsd,df->bsf", x, w_up)
        h = _gelu(g) * u
        return torch.einsum("bsf,fd->bsd", h, w_down)

    x = A_x
    h = x + _A_ref(_layernorm(x, axis=-1), A_wqkv, A_wo)
    h2 = h + _B_ref(_layernorm(h, axis=-1), B_w_gate, B_w_up, B_w_down)
    return h2
```

```cpp
#include <hip/hip_runtime.h>
#include <hip/hip_bf16.h>
#include <stdint.h>

#define LAS __attribute__((address_space(3)))
#define GAS __attribute__((address_space(1)))
#define DEVI __device__ __forceinline__

typedef __attribute__((ext_vector_type(8))) __bf16 bf16x8;
typedef __attribute__((ext_vector_type(4))) __bf16 bf16x4;
typedef __attribute__((ext_vector_type(2))) __bf16 bf16x2;
typedef __attribute__((ext_vector_type(4))) short s16x4;
typedef __attribute__((ext_vector_type(8))) short s16x8;
typedef __attribute__((ext_vector_type(4))) float f32x4;
typedef __attribute__((ext_vector_type(2))) float f32x2;
typedef __attribute__((ext_vector_type(4))) unsigned u32x4;
typedef __attribute__((ext_vector_type(2))) unsigned u32x2;
typedef unsigned short u16;

constexpr int SEQ = 2048, DM = 1024, NH = 32, DH = 32, DF = 3584;
constexpr float LN_EPS = 1e-5f;

DEVI unsigned pack_bf16(float lo, float hi) {
    bf16x2 v; v.x = (__bf16)lo; v.y = (__bf16)hi;
    return __builtin_bit_cast(unsigned, v);
}
DEVI float bf_lo(unsigned u) { return __uint_as_float(u << 16); }
DEVI float bf_hi(unsigned u) { return __uint_as_float(u & 0xffff0000u); }
DEVI float round_bf16(float x) { return (float)(__bf16)x; }

DEVI void glds16(const char* sbase, unsigned voff, unsigned lds_dst) {
    asm volatile("s_mov_b32 m0, %2\n\ts_nop 0\n\tglobal_load_lds_dwordx4 %0, %1"
                 :
                 : "v"(voff), "s"(sbase), "s"(lds_dst)
                 : "memory");
}
DEVI bf16x8 lds_read_b128(LAS char* p) { return *(LAS bf16x8*)p; }
DEVI bf16x8 lds_read_tr2(LAS char* p0, LAS char* p1) {
    s16x4 a = __builtin_amdgcn_ds_read_tr16_b64_v4i16((LAS s16x4*)p0);
    s16x4 b = __builtin_amdgcn_ds_read_tr16_b64_v4i16((LAS s16x4*)p1);
    s16x8 r = __builtin_shufflevector(a, b, 0, 1, 2, 3, 4, 5, 6, 7);
    return __builtin_bit_cast(bf16x8, r);
}
DEVI f32x4 mfma16(bf16x8 a, bf16x8 b, f32x4 c) { return __builtin_amdgcn_mfma_f32_16x16x32_bf16(a, b, c, 0, 0, 0); }

template <int N> DEVI void wait_vm() { asm volatile("s_waitcnt vmcnt(%0)" ::"n"(N) : "memory"); }
DEVI void wait_lgkm0() { asm volatile("s_waitcnt lgkmcnt(0)" ::: "memory"); }
DEVI void bar() {
    asm volatile("" ::: "memory");
    __builtin_amdgcn_s_barrier();
    asm volatile("" ::: "memory");
}
DEVI void sched_fence() { __builtin_amdgcn_sched_barrier(0); }

DEVI float dot2_ones(unsigned v, float acc) {
    const bf16x2 ones = {(__bf16)1.0f, (__bf16)1.0f};
    return __builtin_amdgcn_fdot2_f32_bf16(__builtin_bit_cast(bf16x2, v), ones, acc, false);
}
DEVI float dot2_self(unsigned v, float acc) {
    const bf16x2 a = __builtin_bit_cast(bf16x2, v);
    return __builtin_amdgcn_fdot2_f32_bf16(a, a, acc, false);
}
DEVI float frag_sum(bf16x8 f, float acc) {
    u32x4 u = __builtin_bit_cast(u32x4, f);
    acc = dot2_ones(u.x, acc); acc = dot2_ones(u.y, acc); acc = dot2_ones(u.z, acc); acc = dot2_ones(u.w, acc);
    return acc;
}
DEVI float frag_sq(bf16x8 f, float acc) {
    u32x4 u = __builtin_bit_cast(u32x4, f);
    acc = dot2_self(u.x, acc); acc = dot2_self(u.y, acc); acc = dot2_self(u.z, acc); acc = dot2_self(u.w, acc);
    return acc;
}
DEVI float xor16_32_sum(float v) {
    v += __shfl_xor(v, 16);
    v += __shfl_xor(v, 32);
    return v;
}
DEVI float phi_f(float t) { return (t > 0.f ? t + 1.f : __expf(t)) + 1e-6f; }
DEVI float gelu_tanh(float x) {
    const float u2 = 1.5957691216057308f * (x + 0.044715f * x * x * x);
    return x / (1.f + __expf(-u2));
}

constexpr size_t WS_PQ = 0;
constexpr size_t WS_O = WS_PQ + (size_t)SEQ * DM * 2;
constexpr size_t WS_H = WS_O + (size_t)SEQ * DM * 2;
constexpr size_t WS_HH = WS_H + (size_t)SEQ * DM * 2;
constexpr size_t WS_KVP = WS_HH + (size_t)SEQ * DF * 2;
constexpr size_t WS_ST = WS_KVP + (size_t)16 * NH * 1056 * 4;
constexpr int KVP_STRIDE = 1056;

namespace g1 {
constexpr int BM = 128, BK = 64, S = 4, T = DM / BK;
constexpr int A_BYTES = BM * 128;
constexpr int B_PITCH = 384;
constexpr int B_BYTES = BK * B_PITCH;
constexpr int STAGE = A_BYTES + B_BYTES;
constexpr int NI = 5;
constexpr int LDS_BYTES = S * STAGE;
}

__global__ __launch_bounds__(512, 2) void g1_kernel(const u16* __restrict__ x, const u16* __restrict__ wqkv,
                                                    u16* __restrict__ pq, float* __restrict__ kvp) {
    using namespace g1;
    extern __shared__ __attribute__((aligned(16))) char smem_raw[];
    LAS char* lds = (LAS char*)smem_raw;
    const int tid = threadIdx.x, lane = tid & 63;
    const int w = __builtin_amdgcn_readfirstlane(tid >> 6);
    const int grp = w >> 2, wm = (w >> 1) & 1, wn = w & 1;
    const int bid = blockIdx.x, xcd = bid & 7, loc = bid >> 3;
    const int pm = (xcd & 1) * 8 + (loc & 7);
    const int hp = (xcd >> 1) * 4 + (loc >> 3);
    const int l15 = lane & 15, g = lane >> 4, q4 = (lane >> 2) & 3, p4 = lane & 3;

    unsigned voffA[2], voffB[3];
#pragma unroll
    for (int j = 0; j < 2; ++j) {
        const int i = (j * 8 + w) * 64 + lane, row = i >> 3, c = (i & 7) ^ ((row >> 1) & 7);
        voffA[j] = (unsigned)(row * DM * 2 + c * 16);
    }
#pragma unroll
    for (int j = 0; j < 3; ++j) {
        const int i = (j * 8 + w) * 64 + lane;
        const int row = i / 24, cc = i % 24, blk = cc >> 3, cb = cc & 7;
        const int f = ((row >> 1) & 1) | (((row >> 3) & 1) << 1);
        const int cbs = cb ^ (f << 1);
        voffB[j] = (unsigned)(row * (3 * DM) * 2 + (blk * DM + hp * 64 + cbs * 8) * 2);
    }
    const char* gA = (const char*)x + (size_t)pm * BM * DM * 2;
    const char* gB = (const char*)wqkv;

    const unsigned lds0 = (unsigned)(uintptr_t)lds;
    auto issue = [&](int kt, int buf) {
        const unsigned sb = lds0 + buf * STAGE + w * 1024;
        const char* a = gA + kt * (BK * 2);
        const char* b = gB + (size_t)kt * BK * (3 * DM) * 2;
#pragma unroll
        for (int j = 0; j < 2; ++j) glds16(a, voffA[j], sb + j * 8192);
#pragma unroll
        for (int j = 0; j < 3; ++j) glds16(b, voffB[j], sb + A_BYTES + j * 8192);
    };

    int blk[4];
    blk[0] = 2 * grp + wn; blk[1] = 2 * grp + 1 - wn; blk[2] = 2 * (1 - grp) + wn; blk[3] = 2 * (1 - grp) + 1 - wn;
    int aoff[4];
#pragma unroll
    for (int m = 0; m < 4; ++m) aoff[m] = (64 * wm + 16 * blk[m] + l15) * 128 + 16 * ((4 * grp + g) ^ (l15 >> 1));
    const int fB = ((q4 >> 1) & 1) | ((g & 1) << 1);
    int boff[2];
#pragma unroll
    for (int i = 0; i < 2; ++i)
        boff[i] = A_BYTES + (32 * grp + 8 * g + q4) * B_PITCH + 32 * ((2 * wn + (i ^ wm)) ^ fB) + 8 * p4;

    f32x4 acc[4][6];
#pragma unroll
    for (int m = 0; m < 4; ++m)
#pragma unroll
        for (int n = 0; n < 6; ++n) acc[m][n] = (f32x4){0.f, 0.f, 0.f, 0.f};
    float rs[2] = {0.f, 0.f}, rq[2] = {0.f, 0.f}, cs[3] = {0.f, 0.f, 0.f};

    auto step = [&](int t, auto issue_tag, auto vm_tag) {
        constexpr bool ISSUE = decltype(issue_tag)::value;
        constexpr int VM = decltype(vm_tag)::value;
        LAS char* sb = lds + (t & (S - 1)) * STAGE;
        bf16x8 a[4], b[6];
#pragma unroll
        for (int n = 0; n < 6; ++n) {
            LAS char* pb = sb + boff[n & 1] + (n >> 1) * 128;
            b[n] = lds_read_tr2(pb, pb + 4 * B_PITCH);
        }
#pragma unroll
        for (int m = 0; m < 4; ++m) a[m] = lds_read_b128(sb + aoff[m]);
        if constexpr (ISSUE) issue(t + S - 1, (t + S - 1) & (S - 1));
        cs[0] = frag_sum(b[0], cs[0]); cs[1] = frag_sum(b[2], cs[1]); cs[2] = frag_sum(b[4], cs[2]);
        rs[0] = frag_sum(a[0], rs[0]); rq[0] = frag_sq(a[0], rq[0]);
        rs[1] = frag_sum(a[2], rs[1]); rq[1] = frag_sq(a[2], rq[1]);
        if (grp == 1) wait_vm<VM>();
        wait_lgkm0();
        sched_fence();
        bar();
        sched_fence();
        __builtin_amdgcn_s_setprio(1);
#pragma unroll
        for (int m = 0; m < 4; ++m) {
            acc[m][0] = mfma16(b[0], a[m], acc[m][0]);
            acc[m][1] = mfma16(b[1], a[m], acc[m][1]);
#pragma unroll
            for (int n = 2; n < 6; ++n) acc[m][n] = mfma16(a[m], b[n], acc[m][n]);
        }
        __builtin_amdgcn_s_setprio(0);
        if (grp == 0) wait_vm<VM>();
        sched_fence();
        bar();
        sched_fence();
    };

#pragma unroll
    for (int s = 0; s < S - 1; ++s) issue(s, s);
    wait_vm<(S - 2) * NI>();
    bar();
    if (grp == 1) bar();
    for (int t = 0; t <= T - S; ++t) step(t, std::true_type{}, std::integral_constant<int, (S - 2) * NI>{});
    step(T - 3, std::false_type{}, std::integral_constant<int, 1 * NI>{});
    step(T - 2, std::false_type{}, std::integral_constant<int, 0>{});
    step(T - 1, std::false_type{}, std::integral_constant<int, 0>{});
    if (grp == 0) bar();

    LAS char* xch = lds;
    LAS float* rowstat = (LAS float*)(lds + 98304);
    LAS float* colsum = (LAS float*)(lds + 98304 + 2048);
    {
        LAS char* dst = xch + (w ^ 4) * 12288 + lane * 16;
#pragma unroll
        for (int mm = 0; mm < 2; ++mm)
#pragma unroll
            for (int n = 0; n < 6; ++n) *(LAS f32x4*)(dst + (mm * 6 + n) * 1024) = acc[2 + mm][n];
#pragma unroll
        for (int i = 0; i < 2; ++i) {
            const float s = xor16_32_sum(rs[i]), s2 = xor16_32_sum(rq[i]);
            if (lane < 16) {
                const int row = 64 * wm + 16 * blk[2 * i] + l15;
                rowstat[(grp * 128 + row) * 2] = s;
                rowstat[(grp * 128 + row) * 2 + 1] = s2;
            }
        }
#pragma unroll
        for (int i = 0; i < 3; ++i) {
            const float s = xor16_32_sum(cs[i]);
            if (lane < 16) colsum[grp * 192 + i * 64 + wn * 32 + wm * 16 + l15] = s;
        }
    }
    wait_lgkm0();
    bar();
    f32x4 fin[2][6];
    {
        LAS char* src = xch + w * 12288 + lane * 16;
#pragma unroll
        for (int mm = 0; mm < 2; ++mm)
#pragma unroll
            for (int n = 0; n < 6; ++n) {
                f32x4 v = *(LAS f32x4*)(src + (mm * 6 + n) * 1024);
                fin[mm][n] = v + acc[mm][n];
            }
    }
    auto row_stats = [&](int row, float& mean, float& rstd) {
        const float s = rowstat[row * 2] + rowstat[(128 + row) * 2];
        const float s2 = rowstat[row * 2 + 1] + rowstat[(128 + row) * 2 + 1];
        mean = s * (1.f / DM);
        const float var = fmaxf(s2 * (1.f / DM) - mean * mean, 0.f);
        rstd = rsqrtf(var + LN_EPS);
    };
#pragma unroll
    for (int mm = 0; mm < 2; ++mm) {
        const int row = 64 * wm + 16 * blk[mm] + l15;
        float mean, rstd; row_stats(row, mean, rstd);
#pragma unroll
        for (int i = 0; i < 2; ++i) {
            const int c0 = wn * 32 + (i ^ wm) * 16 + 4 * g;
            float v[4];
#pragma unroll
            for (int j = 0; j < 4; ++j) {
                const float cw = colsum[c0 + j] + colsum[192 + c0 + j];
                v[j] = phi_f(round_bf16(rstd * (fin[mm][i][j] - mean * cw)));
            }
            u32x2 o; o.x = pack_bf16(v[0], v[1]); o.y = pack_bf16(v[2], v[3]);
            *(u32x2*)(pq + (size_t)(pm * BM + row) * DM + hp * 64 + c0) = o;
        }
    }
    float mean_t[2][4], rstd_t[2][4];
#pragma unroll
    for (int mm = 0; mm < 2; ++mm)
#pragma unroll
        for (int j = 0; j < 4; ++j) row_stats(64 * wm + 16 * blk[mm] + 4 * g + j, mean_t[mm][j], rstd_t[mm][j]);
    bf16x8 pk[2], pv[2];
#pragma unroll
    for (int i = 0; i < 2; ++i) {
        const int cc = wn * 32 + (i ^ wm) * 16 + l15;
        const float cwk = colsum[64 + cc] + colsum[192 + 64 + cc];
        const float cwv = colsum[128 + cc] + colsum[192 + 128 + cc];
#pragma unroll
        for (int mm = 0; mm < 2; ++mm)
#pragma unroll
            for (int j = 0; j < 4; ++j) {
                const float kk = round_bf16(rstd_t[mm][j] * (fin[mm][2 + i][j] - mean_t[mm][j] * cwk));
                const float vv = rstd_t[mm][j] * (fin[mm][4 + i][j] - mean_t[mm][j] * cwv);
                pk[i][mm * 4 + j] = (__bf16)phi_f(kk);
                pv[i][mm * 4 + j] = (__bf16)vv;
            }
    }
    bf16x8 ones;
#pragma unroll
    for (int j = 0; j < 8; ++j) ones[j] = (__bf16)1.0f;
    LAS float* red = (LAS float*)(lds + 102400);
    {
        LAS float* mine = red + w * KVP_STRIDE;
#pragma unroll
        for (int i = 0; i < 2; ++i) {
#pragma unroll
            for (int i2 = 0; i2 < 2; ++i2) {
                f32x4 d = mfma16(pk[i], pv[i2], (f32x4){0.f, 0.f, 0.f, 0.f});
                *(LAS f32x4*)(mine + (16 * (i2 ^ wm) + l15) * 32 + 16 * (i ^ wm) + 4 * g) = d;
            }
            f32x4 z = mfma16(pk[i], ones, (f32x4){0.f, 0.f, 0.f, 0.f});
            if (l15 == 0) *(LAS f32x4*)(mine + 1024 + 16 * (i ^ wm) + 4 * g) = z;
        }
    }
    wait_lgkm0();
    bar();
    for (int idx = tid; idx < 2 * 264; idx += 512) {
        const int hh = idx / 264, e4 = idx % 264;
        f32x4 s = *(LAS f32x4*)(red + (hh + 0) * KVP_STRIDE + e4 * 4);
        s += *(LAS f32x4*)(red + (hh + 2) * KVP_STRIDE + e4 * 4);
        s += *(LAS f32x4*)(red + (hh + 4) * KVP_STRIDE + e4 * 4);
        s += *(LAS f32x4*)(red + (hh + 6) * KVP_STRIDE + e4 * 4);
        *(f32x4*)(kvp + ((size_t)pm * NH + hp * 2 + hh) * KVP_STRIDE + e4 * 4) = s;
    }
}

__global__ __launch_bounds__(256) void k2_kernel(const u16* __restrict__ pq, const float* __restrict__ kvp,
                                                 u16* __restrict__ o) {
    __shared__ __attribute__((aligned(16))) u16 kvT[32 * 32 + 32];
    const int tid = threadIdx.x, lane = tid & 63, w = tid >> 6;
    const int h = blockIdx.x & 31, rc = blockIdx.x >> 5;
    const int l15 = lane & 15, g = lane >> 4;
    const int row0 = rc * 256 + w * 64;
    bf16x8 pf[4];
#pragma unroll
    for (int m = 0; m < 4; ++m)
        pf[m] = *(const bf16x8*)(pq + (size_t)(row0 + 16 * m + l15) * DM + h * 32 + 8 * g);
    {
        f32x4 s = {0.f, 0.f, 0.f, 0.f};
        f32x4 v[16];
#pragma unroll
        for (int p = 0; p < 16; ++p) v[p] = *(const f32x4*)(kvp + ((size_t)p * NH + h) * KVP_STRIDE + tid * 4);
#pragma unroll
        for (int p = 0; p < 16; ++p) s += v[p];
        u32x2 r; r.x = pack_bf16(s[0], s[1]); r.y = pack_bf16(s[2], s[3]);
        *(u32x2*)(kvT + tid * 4) = r;
        if (tid < 8) {
            f32x4 z = {0.f, 0.f, 0.f, 0.f};
#pragma unroll
            for (int p = 0; p < 16; ++p) z += *(const f32x4*)(kvp + ((size_t)p * NH + h) * KVP_STRIDE + 1024 + tid * 4);
            u32x2 rz; rz.x = pack_bf16(z[0], z[1]); rz.y = pack_bf16(z[2], z[3]);
            *(u32x2*)(kvT + 1024 + tid * 4) = rz;
        }
    }
    __syncthreads();
    const int e0 = 8 * (l15 >> 2) + (l15 & 3);
    const bf16x8 a0 = *(const bf16x8*)(kvT + e0 * 32 + 8 * g);
    const bf16x8 a1 = *(const bf16x8*)(kvT + (e0 + 4) * 32 + 8 * g);
    const bf16x8 az = *(const bf16x8*)(kvT + 1024 + 8 * g);
    const f32x4 zero = {0.f, 0.f, 0.f, 0.f};
#pragma unroll
    for (int m = 0; m < 4; ++m) {
        const f32x4 n0 = mfma16(a0, pf[m], zero), n1 = mfma16(a1, pf[m], zero), dn = mfma16(az, pf[m], zero);
        const float inv = 1.f / (dn[0] + 1e-6f);
        u32x4 r;
        r.x = pack_bf16(n0[0] * inv, n0[1] * inv); r.y = pack_bf16(n0[2] * inv, n0[3] * inv);
        r.z = pack_bf16(n1[0] * inv, n1[1] * inv); r.w = pack_bf16(n1[2] * inv, n1[3] * inv);
        *(u32x4*)(o + (size_t)(row0 + 16 * m + l15) * DM + h * 32 + 8 * g) = r;
    }
}

namespace g24 {
constexpr int BM = 128, BN = 64, BK = 128, S = 3;
constexpr int A_BYTES = BM * 256;
constexpr int B_BYTES = BK * 128;
constexpr int STAGE = A_BYTES + B_BYTES;
constexpr int NI = 6;
constexpr int LDS_BYTES = S * STAGE;
}

template <int KDIM, bool STATS>
__global__ __launch_bounds__(512, 2) void g24_kernel(const u16* __restrict__ A, const u16* __restrict__ B,
                                                     const u16* __restrict__ res, u16* __restrict__ out,
                                                     float* __restrict__ stats) {
    using namespace g24;
    constexpr int T = KDIM / BK;
    extern __shared__ __attribute__((aligned(16))) char smem_raw[];
    LAS char* lds = (LAS char*)smem_raw;
    const int tid = threadIdx.x, lane = tid & 63;
    const int w = __builtin_amdgcn_readfirstlane(tid >> 6);
    const int grp = w >> 2, kq = w >> 1, wm = w & 1;
    const int bid = blockIdx.x, xcd = bid & 7, loc = bid >> 3;
    const int pm = (xcd & 3) * 4 + (loc & 3);
    const int pn = (xcd >> 2) * 8 + (loc >> 2);
    const int l15 = lane & 15, g = lane >> 4, q4 = (lane >> 2) & 3, p4 = lane & 3;

    unsigned voffA[4], voffB[2];
#pragma unroll
    for (int j = 0; j < 4; ++j) {
        const int i = (j * 8 + w) * 64 + lane, row = i >> 4, c = (i & 15) ^ (row & 15);
        voffA[j] = (unsigned)(row * KDIM * 2 + c * 16);
    }
#pragma unroll
    for (int j = 0; j < 2; ++j) {
        const int i = (j * 8 + w) * 64 + lane, row = i >> 3, cb = i & 7;
        const int f = ((row >> 1) & 1) | (((row >> 3) & 1) << 1);
        voffB[j] = (unsigned)(row * DM * 2 + (pn * 64 + (cb ^ (f << 1)) * 8) * 2);
    }
    const char* gA = (const char*)A + (size_t)pm * BM * KDIM * 2;
    const char* gB = (const char*)B;
    const unsigned lds0 = (unsigned)(uintptr_t)lds;
    auto issue = [&](int kt, int buf) {
        const unsigned sb = lds0 + buf * STAGE + w * 1024;
        const char* a = gA + kt * (BK * 2);
        const char* b = gB + (size_t)kt * BK * DM * 2;
#pragma unroll
        for (int j = 0; j < 4; ++j) glds16(a, voffA[j], sb + j * 8192);
#pragma unroll
        for (int j = 0; j < 2; ++j) glds16(b, voffB[j], sb + A_BYTES + j * 8192);
    };
    int aoff[4];
#pragma unroll
    for (int m = 0; m < 4; ++m) aoff[m] = (64 * wm + 16 * ((m + kq) & 3) + l15) * 256 + 16 * ((4 * kq + g) ^ l15);
    const int fB = ((q4 >> 1) & 1) | ((g & 1) << 1);
    int boff[4];
#pragma unroll
    for (int i = 0; i < 4; ++i) boff[i] = A_BYTES + (32 * kq + 8 * g + q4) * 128 + 32 * (i ^ fB) + 8 * p4;

    f32x4 acc[4][4];
#pragma unroll
    for (int m = 0; m < 4; ++m)
#pragma unroll
        for (int n = 0; n < 4; ++n) acc[m][n] = (f32x4){0.f, 0.f, 0.f, 0.f};

    int buf_rd = 0, buf_wr = S - 1;
    auto step = [&](int t, auto issue_tag, auto vm_tag) {
        constexpr bool ISSUE = decltype(issue_tag)::value;
        constexpr int VM = decltype(vm_tag)::value;
        LAS char* sb = lds + buf_rd * STAGE;
        bf16x8 a[4], b[4];
#pragma unroll
        for (int n = 0; n < 4; ++n) b[n] = lds_read_tr2(sb + boff[n], sb + boff[n] + 4 * 128);
#pragma unroll
        for (int m = 0; m < 4; ++m) a[m] = lds_read_b128(sb + aoff[m]);
        if constexpr (ISSUE) issue(t + S - 1, buf_wr);
        buf_rd = buf_rd == S - 1 ? 0 : buf_rd + 1;
        buf_wr = buf_wr == S - 1 ? 0 : buf_wr + 1;
        if (grp == 1) wait_vm<VM>();
        wait_lgkm0();
        sched_fence();
        bar();
        sched_fence();
        __builtin_amdgcn_s_setprio(1);
#pragma unroll
        for (int m = 0; m < 4; ++m)
#pragma unroll
            for (int n = 0; n < 4; ++n) acc[m][n] = mfma16(b[n], a[m], acc[m][n]);
        __builtin_amdgcn_s_setprio(0);
        if (grp == 0) wait_vm<VM>();
        sched_fence();
        bar();
        sched_fence();
    };
#pragma unroll
    for (int s = 0; s < S - 1; ++s) issue(s, s);
    wait_vm<(S - 2) * NI>();
    bar();
    if (grp == 1) bar();
    for (int t = 0; t <= T - S; ++t) step(t, std::true_type{}, std::integral_constant<int, (S - 2) * NI>{});
    step(T - 2, std::false_type{}, std::integral_constant<int, 0>{});
    step(T - 1, std::false_type{}, std::integral_constant<int, 0>{});
    if (grp == 0) bar();

    {
#pragma unroll
        for (int m = 1; m < 4; ++m) {
            const int dw = ((m + kq) & 3) * 2 + wm;
            LAS char* dst = lds + ((dw * 3 + (m - 1)) * 4) * 1024 + lane * 16;
#pragma unroll
            for (int n = 0; n < 4; ++n) *(LAS f32x4*)(dst + n * 1024) = acc[m][n];
        }
    }
    const int row = pm * BM + 64 * wm + 16 * kq + l15;
    u32x2 rv[4];
#pragma unroll
    for (int n = 0; n < 4; ++n) rv[n] = *(const u32x2*)(res + (size_t)row * DM + pn * 64 + 16 * n + 4 * g);
    wait_lgkm0();
    bar();
    f32x4 fin[4];
#pragma unroll
    for (int n = 0; n < 4; ++n) fin[n] = acc[0][n];
#pragma unroll
    for (int s = 0; s < 3; ++s) {
        LAS char* src = lds + ((w * 3 + s) * 4) * 1024 + lane * 16;
#pragma unroll
        for (int n = 0; n < 4; ++n) fin[n] += *(LAS f32x4*)(src + n * 1024);
    }
    float hv[4][4];
#pragma unroll
    for (int n = 0; n < 4; ++n) {
        hv[n][0] = round_bf16(bf_lo(rv[n].x) + round_bf16(fin[n][0]));
        hv[n][1] = round_bf16(bf_hi(rv[n].x) + round_bf16(fin[n][1]));
        hv[n][2] = round_bf16(bf_lo(rv[n].y) + round_bf16(fin[n][2]));
        hv[n][3] = round_bf16(bf_hi(rv[n].y) + round_bf16(fin[n][3]));
        u32x2 o; o.x = pack_bf16(hv[n][0], hv[n][1]); o.y = pack_bf16(hv[n][2], hv[n][3]);
        *(u32x2*)(out + (size_t)row * DM + pn * 64 + 16 * n + 4 * g) = o;
    }
    if constexpr (STATS) {
        float s = 0.f;
#pragma unroll
        for (int n = 0; n < 4; ++n) s += (hv[n][0] + hv[n][1]) + (hv[n][2] + hv[n][3]);
        s = xor16_32_sum(s);
        const float mu = s * (1.f / 64.f);
        float m2 = 0.f;
#pragma unroll
        for (int n = 0; n < 4; ++n)
#pragma unroll
            for (int j = 0; j < 4; ++j) { const float d = hv[n][j] - mu; m2 += d * d; }
        m2 = xor16_32_sum(m2);
        if (lane < 16) *(f32x2*)(stats + ((size_t)row * 16 + pn) * 2) = (f32x2){mu, m2};
    }
}

namespace g3 {
constexpr int BM = 256, BK = 64, T = DM / BK;
constexpr int A_BYTES = BM * 128;
constexpr int SUB = 64 * 224;
constexpr int B_BYTES = 2 * SUB;
constexpr int STAGE = A_BYTES + B_BYTES;
constexpr int ST_OFF = 2 * STAGE;
constexpr int LDS_BYTES = ST_OFF + 32768;
}

__global__ __launch_bounds__(512, 2) void g3_kernel(const u16* __restrict__ h, const u16* __restrict__ wg,
                                                    const u16* __restrict__ wu, const float* __restrict__ stats,
                                                    u16* __restrict__ hh) {
    using namespace g3;
    extern __shared__ __attribute__((aligned(16))) char smem_raw[];
    LAS char* lds = (LAS char*)smem_raw;
    const int tid = threadIdx.x, lane = tid & 63;
    const int w = __builtin_amdgcn_readfirstlane(tid >> 6);
    const int grp = w >> 2, wm = w >> 1, wn = w & 1;
    const int bid = blockIdx.x, xcd = bid & 7, loc = bid >> 3;
    const int pm = (xcd & 1) * 4 + (loc & 3);
    const int pn = (xcd >> 1) * 8 + (loc >> 2);
    const int l15 = lane & 15, g = lane >> 4, q4 = (lane >> 2) & 3, p4 = lane & 3;

    unsigned voffA[4], voffB[4];
    const char* gBw[4];
    int ldsB[4];
#pragma unroll
    for (int j = 0; j < 4; ++j) {
        const int i = (j * 8 + w) * 64 + lane, row = i >> 3, c = (i & 7) ^ ((row >> 1) & 7);
        voffA[j] = (unsigned)(row * DM * 2 + c * 16);
    }
#pragma unroll
    for (int j = 0; j < 4; ++j) {
        int b = grp == 0 ? w * 4 + j : 16 + (w - 4) * 3 + j;
        if (b > 27) b = 27;
        const int sub = b >= 14 ? 1 : 0;
        const int i = (b - sub * 14) * 64 + lane, rho = i / 14, cc = i % 14;
        const int r = (rho & ~12) | ((rho & 4) << 1) | ((rho & 8) >> 1);
        voffB[j] = (unsigned)(r * DF * 2 + (pn * 112 + cc * 8) * 2);
        gBw[j] = (const char*)(sub ? wu : wg);
        ldsB[j] = A_BYTES + b * 1024;
    }
    const char* gA = (const char*)h + (size_t)pm * BM * DM * 2;
    const unsigned lds0 = (unsigned)(uintptr_t)lds;
    auto issue = [&](int kt, int buf) {
        const unsigned sb = lds0 + buf * STAGE;
        const char* a = gA + kt * (BK * 2);
        const size_t bo = (size_t)kt * BK * DF * 2;
#pragma unroll
        for (int j = 0; j < 4; ++j) glds16(a, voffA[j], sb + (j * 8 + w) * 1024);
#pragma unroll
        for (int j = 0; j < 3; ++j) glds16(gBw[j] + bo, voffB[j], sb + ldsB[j]);
        if (grp == 0) glds16(gBw[3] + bo, voffB[3], sb + ldsB[3]);
    };
    int aoff[2];
#pragma unroll
    for (int ks = 0; ks < 2; ++ks) aoff[ks] = (64 * wm + l15) * 128 + 16 * ((4 * ks + g) ^ (l15 >> 1));
    const int brow = (16 * (g >> 1) + 4 * (g & 1) + q4) * 224;
    int pr[3], boff[3];
#pragma unroll
    for (int j = 0; j < 3; ++j) { pr[j] = (j + wm) % 3; boff[j] = A_BYTES + brow + 112 * wn + 8 * p4 + 32 * pr[j]; }
    const int boffX = A_BYTES + brow + 112 * wn + 96 + (p4 < 2 ? 8 * p4 : SUB + 8 * (p4 - 2));

    f32x4 acc[4][7];
#pragma unroll
    for (int m = 0; m < 4; ++m)
#pragma unroll
        for (int n = 0; n < 7; ++n) acc[m][n] = (f32x4){0.f, 0.f, 0.f, 0.f};
    float cs[3] = {0.f, 0.f, 0.f};

    auto unit = [&](int t, auto ks_tag, auto issue_tag, auto wait_tag) {
        constexpr int ks = decltype(ks_tag)::value;
        constexpr bool ISSUE = decltype(issue_tag)::value;
        constexpr bool WAIT = decltype(wait_tag)::value;
        LAS char* sb = lds + (t & 1) * STAGE;
        bf16x8 a[4], b[7];
#pragma unroll
        for (int n = 0; n < 6; ++n) {
            LAS char* pb = sb + boff[n % 3] + (n >= 3 ? SUB : 0) + ks * 32 * 224;
            b[n] = lds_read_tr2(pb, pb + 8 * 224);
        }
        { LAS char* pb = sb + boffX + ks * 32 * 224; b[6] = lds_read_tr2(pb, pb + 8 * 224); }
#pragma unroll
        for (int m = 0; m < 4; ++m) a[m] = lds_read_b128(sb + aoff[ks] + m * 2048);
        if constexpr (ISSUE) issue(t + 1, (t + 1) & 1);
        if (wm == 3) cs[2] = frag_sum(b[6], cs[2]);
        else { cs[0] = frag_sum(b[0], cs[0]); cs[1] = frag_sum(b[3], cs[1]); }
        if constexpr (WAIT) { if (grp == 1) wait_vm<0>(); }
        wait_lgkm0();
        sched_fence();
        bar();
        sched_fence();
        __builtin_amdgcn_s_setprio(1);
#pragma unroll
        for (int m = 0; m < 4; ++m)
#pragma unroll
            for (int n = 0; n < 7; ++n) acc[m][n] = mfma16(b[n], a[m], acc[m][n]);
        __builtin_amdgcn_s_setprio(0);
        if constexpr (WAIT) { if (grp == 0) wait_vm<0>(); }
        sched_fence();
        bar();
        sched_fence();
    };
    {
        const char* gs = (const char*)stats + (size_t)pm * BM * 128;
#pragma unroll
        for (int j = 0; j < 4; ++j) glds16(gs, ((j * 8 + w) * 64 + lane) * 16, lds0 + ST_OFF + (j * 8 + w) * 1024);
    }
    issue(0, 0);
    wait_vm<0>();
    bar();
    if (grp == 1) bar();
    for (int t = 0; t < T - 1; ++t) {
        unit(t, std::integral_constant<int, 0>{}, std::true_type{}, std::false_type{});
        unit(t, std::integral_constant<int, 1>{}, std::false_type{}, std::true_type{});
    }
    unit(T - 1, std::integral_constant<int, 0>{}, std::false_type{}, std::false_type{});
    unit(T - 1, std::integral_constant<int, 1>{}, std::false_type{}, std::false_type{});
    if (grp == 0) bar();

    LAS float* colsum = (LAS float*)lds;
    LAS f32x2* rstat = (LAS f32x2*)(lds + 1024);
    {
        const float s0 = xor16_32_sum(cs[0]), s1 = xor16_32_sum(cs[1]), s2 = xor16_32_sum(cs[2]);
        if (lane < 16) {
            if (wm < 3) {
                colsum[(wn * 7 + wm) * 16 + l15] = s0;
                colsum[(wn * 7 + wm + 3) * 16 + l15] = s1;
            } else {
                colsum[(wn * 7 + 6) * 16 + l15] = s2;
            }
        }
        if (tid < 256) {
            LAS f32x4* sp = (LAS f32x4*)(lds + ST_OFF + tid * 128);
            float mu[16], m2 = 0.f, ms = 0.f;
#pragma unroll
            for (int i = 0; i < 8; ++i) {
                const f32x4 v = sp[i];
                mu[2 * i] = v[0]; mu[2 * i + 1] = v[2];
                m2 += v[1] + v[3]; ms += v[0] + v[2];
            }
            const float mean = ms * (1.f / 16.f);
            float dd = 0.f;
#pragma unroll
            for (int i = 0; i < 16; ++i) { const float d = mu[i] - mean; dd += d * d; }
            const float var = (m2 + 64.f * dd) * (1.f / DM);
            rstat[tid] = (f32x2){mean, rsqrtf(var + LN_EPS)};
        }
    }
    wait_lgkm0();
    bar();
    f32x4 cw[7];
#pragma unroll
    for (int n = 0; n < 7; ++n)
        cw[n] = *(LAS f32x4*)(colsum + (wn * 7 + (n < 6 ? pr[n % 3] + (n / 3) * 3 : 6)) * 16 + 4 * g);
    f32x2 st[4];
#pragma unroll
    for (int m = 0; m < 4; ++m) st[m] = rstat[64 * wm + 16 * m + l15];
    const size_t colbase = (size_t)pn * 112 + 56 * wn;
#pragma unroll
    for (int m = 0; m < 4; ++m) {
        const int row = pm * BM + 64 * wm + 16 * m + l15;
#pragma unroll
        for (int n = 0; n < 3; ++n) {
            float v[4];
#pragma unroll
            for (int j = 0; j < 4; ++j) {
                const float gg = round_bf16(st[m].y * (acc[m][n][j] - st[m].x * cw[n][j]));
                const float uu = round_bf16(st[m].y * (acc[m][n + 3][j] - st[m].x * cw[n + 3][j]));
                v[j] = round_bf16(gelu_tanh(gg)) * uu;
            }
            u32x2 o; o.x = pack_bf16(v[0], v[1]); o.y = pack_bf16(v[2], v[3]);
            *(u32x2*)(hh + (size_t)row * DF + colbase + 16 * pr[n] + 4 * g) = o;
        }
    }
#pragma unroll
    for (int mp = 0; mp < 2; ++mp) {
        float v[4];
#pragma unroll
        for (int j = 0; j < 4; ++j) {
            const float y0 = round_bf16(st[2 * mp].y * (acc[2 * mp][6][j] - st[2 * mp].x * cw[6][j]));
            const float y1 = round_bf16(st[2 * mp + 1].y * (acc[2 * mp + 1][6][j] - st[2 * mp + 1].x * cw[6][j]));
            auto r = __builtin_amdgcn_permlane32_swap(__float_as_uint(y0), __float_as_uint(y1), false, false);
            v[j] = round_bf16(gelu_tanh(__uint_as_float(r[0]))) * __uint_as_float(r[1]);
        }
        const int row = pm * BM + 64 * wm + 16 * (2 * mp + (lane >> 5)) + l15;
        u32x2 o; o.x = pack_bf16(v[0], v[1]); o.y = pack_bf16(v[2], v[3]);
        *(u32x2*)(hh + (size_t)row * DF + colbase + 48 + 4 * (g & 1)) = o;
    }
}

extern "C" void kernel_launch(void* const* d_in, const int* in_sizes, int n_in, void* d_out, int out_size,
                              void* d_ws, size_t ws_size, hipStream_t stream) {
    const u16* x = (const u16*)d_in[0];
    const u16* wqkv = (const u16*)d_in[1];
    const u16* wo = (const u16*)d_in[2];
    const u16* wg = (const u16*)d_in[4];
    const u16* wu = (const u16*)d_in[5];
    const u16* wd = (const u16*)d_in[6];
    char* ws = (char*)d_ws;
    u16* pq = (u16*)(ws + WS_PQ);
    u16* o = (u16*)(ws + WS_O);
    u16* h = (u16*)(ws + WS_H);
    u16* hh = (u16*)(ws + WS_HH);
    float* kvp = (float*)(ws + WS_KVP);
    float* st = (float*)(ws + WS_ST);
    u16* out = (u16*)d_out;

    (void)hipFuncSetAttribute((const void*)g1_kernel, hipFuncAttributeMaxDynamicSharedMemorySize, g1::LDS_BYTES);
    (void)hipFuncSetAttribute((const void*)g24_kernel<DM, true>, hipFuncAttributeMaxDynamicSharedMemorySize, g24::LDS_BYTES);
    (void)hipFuncSetAttribute((const void*)g24_kernel<DF, false>, hipFuncAttributeMaxDynamicSharedMemorySize, g24::LDS_BYTES);
    (void)hipFuncSetAttribute((const void*)g3_kernel, hipFuncAttributeMaxDynamicSharedMemorySize, g3::LDS_BYTES);

    g1_kernel<<<256, 512, g1::LDS_BYTES, stream>>>(x, wqkv, pq, kvp);
    k2_kernel<<<256, 256, 0, stream>>>(pq, kvp, o);
    g24_kernel<DM, true><<<256, 512, g24::LDS_BYTES, stream>>>(o, wo, x, h, st);
    g3_kernel<<<256, 512, g3::LDS_BYTES, stream>>>(h, wg, wu, st, hh);
    g24_kernel<DF, false><<<256, 512, g24::LDS_BYTES, stream>>>(hh, wd, h, out, nullptr);
}
```

```cpp
#include <hip/hip_runtime.h>
#include <hip/hip_bf16.h>
#include <stdint.h>
#include <type_traits>

#define LAS __attribute__((address_space(3)))
#define DEVI __device__ __forceinline__
#define IC(n) std::integral_constant<int, (n)>{}

typedef __attribute__((ext_vector_type(8))) __bf16 bf16x8;
typedef __attribute__((ext_vector_type(2))) __bf16 bf16x2;
typedef __attribute__((ext_vector_type(4))) short s16x4;
typedef __attribute__((ext_vector_type(8))) short s16x8;
typedef __attribute__((ext_vector_type(4))) float f32x4;
typedef __attribute__((ext_vector_type(2))) float f32x2;
typedef __attribute__((ext_vector_type(4))) unsigned u32x4;
typedef __attribute__((ext_vector_type(2))) unsigned u32x2;
typedef unsigned short u16;
typedef unsigned long long u64;
typedef __amdgpu_buffer_rsrc_t rsrc_t;

constexpr int SEQ = 2048, DM = 1024, NH = 32, DF = 3584;
constexpr float LN_EPS = 1e-5f;
constexpr int LDS_BYTES = 163840;
constexpr int SC1 = 16;

constexpr unsigned FL_EPOCH = 0;
constexpr unsigned FL_F1R = 1024;
constexpr unsigned FL_F1C = FL_F1R + 2048;
constexpr unsigned FL_F2 = FL_F1C + 2048;
constexpr unsigned FL_F3 = FL_F2 + 2048;
constexpr unsigned FL_F4 = FL_F3 + 2048;
constexpr unsigned FL_F6 = FL_F4 + 2048;
constexpr unsigned FL_F7 = FL_F6 + 2048;
constexpr unsigned WS_O = 65536;
constexpr unsigned WS_H = WS_O + SEQ * DM * 2;
constexpr unsigned WS_HH = WS_H + SEQ * DM * 2;
constexpr unsigned WS_KVP = WS_HH + SEQ * DF * 2;
constexpr unsigned WS_RS1 = WS_KVP + 256 * 2 * 1056 * 4;
constexpr unsigned WS_CS1 = WS_RS1 + 16 * 16 * 128 * 8;
constexpr unsigned WS_ST2 = WS_CS1 + 16 * 16 * 192 * 4;
constexpr unsigned WS_X4 = WS_ST2 + SEQ * 16 * 8;
constexpr unsigned WS_CS3 = WS_X4 + 256 * 32768;
constexpr unsigned WS_END = WS_CS3 + 4 * 7168 * 4;
constexpr unsigned SENT = 0xffffffffu;

DEVI unsigned pack_bf16(float lo, float hi) {
    bf16x2 v; v.x = (__bf16)lo; v.y = (__bf16)hi;
    return __builtin_bit_cast(unsigned, v);
}
DEVI float bf_lo(unsigned u) { return __uint_as_float(u << 16); }
DEVI float bf_hi(unsigned u) { return __uint_as_float(u & 0xffff0000u); }
DEVI float round_bf16(float x) { return (float)(__bf16)x; }

template <int OFF> DEVI void glds(const char* sbase, unsigned voff, unsigned lds_dst) {
    asm volatile("s_mov_b32 m0, %2\n\ts_nop 0\n\tglobal_load_lds_dwordx4 %0, %1 offset:%3 sc1"
                 :
                 : "v"(voff), "s"(sbase), "s"(lds_dst), "n"(OFF)
                 : "memory");
}
DEVI bf16x8 lds_b128(LAS char* p) { return *(LAS bf16x8*)p; }
DEVI bf16x8 lds_tr2(LAS char* p0, LAS char* p1) {
    s16x4 a = __builtin_amdgcn_ds_read_tr16_b64_v4i16((LAS s16x4*)p0);
    s16x4 b = __builtin_amdgcn_ds_read_tr16_b64_v4i16((LAS s16x4*)p1);
    s16x8 r = __builtin_shufflevector(a, b, 0, 1, 2, 3, 4, 5, 6, 7);
    return __builtin_bit_cast(bf16x8, r);
}
DEVI f32x4 mfma16(bf16x8 a, bf16x8 b, f32x4 c) { return __builtin_amdgcn_mfma_f32_16x16x32_bf16(a, b, c, 0, 0, 0); }
template <int N> DEVI void wait_vm() { asm volatile("s_waitcnt vmcnt(%0)" ::"n"(N) : "memory"); }
DEVI void wait_lgkm0() { asm volatile("s_waitcnt lgkmcnt(0)" ::: "memory"); }
DEVI void bar() {
    asm volatile("" ::: "memory");
    __builtin_amdgcn_s_barrier();
    asm volatile("" ::: "memory");
}
DEVI void fence() { __builtin_amdgcn_sched_barrier(0); }
DEVI float xor16_32_sum(float v) { v += __shfl_xor(v, 16); v += __shfl_xor(v, 32); return v; }
DEVI float phi_f(float t) { return (t > 0.f ? t + 1.f : __expf(t)) + 1e-6f; }
DEVI bf16x8 ones8() {
    bf16x8 o;
#pragma unroll
    for (int j = 0; j < 8; ++j) o[j] = (__bf16)1.0f;
    return o;
}
DEVI void combine8(const f32x4 (&v)[4], float& mean, float& m2) {
    const float ms = ((v[0][0] + v[0][2]) + (v[1][0] + v[1][2])) + ((v[2][0] + v[2][2]) + (v[3][0] + v[3][2]));
    mean = ms * 0.125f;
    float dd = 0.f, q = 0.f;
#pragma unroll
    for (int i = 0; i < 4; ++i) {
        const float d0 = v[i][0] - mean, d1 = v[i][2] - mean;
        dd += d0 * d0 + d1 * d1;
        q += v[i][1] + v[i][3];
    }
    m2 = q + 64.f * dd;
}
DEVI u32x4 ld128(rsrc_t rs, unsigned off) { return __builtin_amdgcn_raw_buffer_load_b128(rs, off, 0, SC1); }
DEVI void st128(rsrc_t rs, unsigned off, u32x4 v) { __builtin_amdgcn_raw_buffer_store_b128(v, rs, off, 0, SC1); }
DEVI void st64(rsrc_t rs, unsigned off, u32x2 v) { __builtin_amdgcn_raw_buffer_store_b64(v, rs, off, 0, SC1); }
DEVI void st32(rsrc_t rs, unsigned off, unsigned v) { __builtin_amdgcn_raw_buffer_store_b32(v, rs, off, 0, SC1); }
DEVI u32x2 ld64(rsrc_t rs, unsigned off) { return __builtin_amdgcn_raw_buffer_load_b64(rs, off, 0, SC1); }
DEVI unsigned ld32(rsrc_t rs, unsigned off) { return __builtin_amdgcn_raw_buffer_load_b32(rs, off, 0, SC1); }
DEVI u32x4 no_sent(f32x4 v) {
    u32x4 u = __builtin_bit_cast(u32x4, v);
#pragma unroll
    for (int j = 0; j < 4; ++j) u[j] = min(u[j], SENT - 1u);
    return u;
}
DEVI unsigned umax4(u32x4 v) { return max(max(v[0], v[1]), max(v[2], v[3])); }
DEVI bool timed_out(u64 t0) { return __builtin_amdgcn_s_memrealtime() - t0 > 1000000ull; }

DEVI void wait_flags(rsrc_t rs, unsigned off, u32x2 tag) {
    const u64 t0 = __builtin_amdgcn_s_memrealtime();
    for (;;) {
        const u32x2 v = ld64(rs, off);
        if (__all(v.x == tag.x && v.y == tag.y)) break;
        if (__builtin_amdgcn_s_memrealtime() - t0 > 1000000ull) break;
        __builtin_amdgcn_s_sleep(1);
    }
    asm volatile("" ::: "memory");
}

#define STAMP(kid, i)

struct Ctx {
    LAS char* lds;
    unsigned lds0;
    rsrc_t rs;
    const char* ws;
    u32x2 tag;
    int tid, lane, w, bid;
};
template <int N> DEVI void finish_tag(Ctx& c) {
    asm volatile("s_waitcnt vmcnt(%1)" : "+v"(c.tag) : "n"(N) : "memory");
    const unsigned ex = __builtin_amdgcn_readfirstlane(c.tag.x), ey = __builtin_amdgcn_readfirstlane(c.tag.y);
    c.tag.x = ex + 1u;
    c.tag.y = ey ^ 0x9e3779b9u;
}
DEVI void asm_load4(rsrc_t rs, unsigned voff, f32x4 (&v)[4]) {
    asm volatile("buffer_load_dwordx4 %0, %4, %5, 0 offen sc1\n\tbuffer_load_dwordx4 %1, %4, %5, 0 offen offset:16 sc1\n\t"
                 "buffer_load_dwordx4 %2, %4, %5, 0 offen offset:32 sc1\n\tbuffer_load_dwordx4 %3, %4, %5, 0 offen offset:48 sc1"
                 : "=&v"(v[0]), "=&v"(v[1]), "=&v"(v[2]), "=&v"(v[3])
                 : "v"(voff), "s"(rs)
                 : "memory");
}
template <int N> DEVI void wait_regs4(f32x4 (&v)[4]) {
    asm volatile("s_waitcnt vmcnt(%4)" : "+v"(v[0]), "+v"(v[1]), "+v"(v[2]), "+v"(v[3]) : "n"(N) : "memory");
}

namespace g2 { constexpr int B_RING = 65536, B_PIECE = 8192; }
namespace g4 { constexpr int B_RING = 81920, B_PIECE = 16384; }
namespace g3 {
constexpr int A_PIECE = 16384, B_SUB = 32 * 224, B_PIECE = 2 * B_SUB;
constexpr int NR = 5;
constexpr int B_OFF = NR * A_PIECE;
constexpr int TAB_OFF = B_OFF + NR * B_PIECE;
constexpr int SCR_OFF = TAB_OFF + 4096 + 1024;
constexpr int OPITCH = 240;
constexpr unsigned BSTEP = 32 * DF * 2, AHALF = 128 * DM * 2;
}

template <bool G4> struct RsGeom {
    int pm, pn, ks;
    unsigned voffB[2];
    const char* gB;
    DEVI RsGeom(const Ctx& c, const u16* W) {
        const int xcd = c.bid & 7, loc = c.bid >> 3;
        if constexpr (G4) {
            ks = xcd & 1; pm = (xcd >> 1) * 4 + (loc & 3); pn = loc >> 2;
#pragma unroll
            for (int j = 0; j < 2; ++j) {
                const int row = 4 * (2 * c.w + j) + (c.lane >> 4), ch = c.lane & 15;
                const int s = (row & 3) | (((row >> 3) & 1) << 2);
                voffB[j] = (unsigned)(row * 2048 + pn * 256 + ((ch ^ (s << 1)) * 16) - j * 1024);
            }
            gB = (const char*)W + (size_t)ks * 1792 * 2048;
        } else {
            ks = 0; pm = (xcd & 3) * 4 + (loc & 3); pn = (xcd >> 2) * 8 + (loc >> 2);
            const int row = 8 * c.w + (c.lane >> 3), cb = c.lane & 7;
            const int f = ((row >> 1) & 1) | (((row >> 3) & 1) << 1);
            voffB[0] = (unsigned)(row * 2048 + pn * 128 + ((cb ^ (f << 1)) * 16));
            voffB[1] = 0;
            gB = (const char*)W;
        }
    }
    DEVI void pieceB(const Ctx& c, int p) const {
        const char* b = gB + (size_t)p * (64 * 2048);
        if constexpr (G4) {
            const unsigned d = c.lds0 + g4::B_RING + (p & 3) * g4::B_PIECE + 2 * c.w * 1024;
            glds<0>(b, voffB[0], d); glds<1024>(b, voffB[1], d);
        } else {
            glds<0>(b, voffB[0], c.lds0 + g2::B_RING + (p % 12) * g2::B_PIECE + c.w * 1024);
        }
    }
};

struct G3Geom {
    int pm, pn;
    unsigned voffB[2];
    const char* gB[2];
    unsigned ldsB, ringmask;
    DEVI G3Geom(const Ctx& c, const u16* wg, const u16* wu, const u16* wd) {
        using namespace g3;
        const int xcd = c.bid & 7, loc = c.bid >> 3;
        pm = (xcd & 1) * 4 + (loc & 3); pn = (xcd >> 1) * 8 + (loc >> 2);
        if (c.w < 7) {
#pragma unroll
            for (int j = 0; j < 2; ++j) {
                const int b = 2 * c.w + j, sub = b >= 7 ? 1 : 0;
                const int i = (b - 7 * sub) * 64 + c.lane, rho = i / 14, cc = i % 14;
                const int r = (rho & ~12) | ((rho & 4) << 1) | ((rho & 8) >> 1);
                voffB[j] = (unsigned)(r * DF * 2 + (pn * 112 + cc * 8) * 2);
                gB[j] = (const char*)(sub ? wu : wg) - j * 1024;
            }
            ldsB = c.lds0 + B_OFF + 2 * c.w * 1024;
            ringmask = ~0u;
        } else {
            const int l0 = min(c.lane, 3), l1 = 4 + min(c.lane, 2);
            voffB[0] = (unsigned)((c.bid * 7 + l0) * 128);
            voffB[1] = (unsigned)((c.bid * 7 + l1) * 128);
            gB[0] = (const char*)wd; gB[1] = (const char*)wd - 1024;
            ldsB = c.lds0 + SCR_OFF;
            ringmask = 0u;
        }
    }
    DEVI void prefetchB(const Ctx& c) const {
#pragma unroll
        for (int p = 0; p < 4; ++p) {
            const unsigned d = ldsB + ((unsigned)(p * g3::B_PIECE) & ringmask);
            glds<0>(gB[0] + (size_t)p * g3::BSTEP, voffB[0], d);
            glds<1024>(gB[1] + (size_t)p * g3::BSTEP, voffB[1], d);
        }
    }
};

namespace g1 {
constexpr int A_BYTES = 16384, B_PITCH = 384, STAGE = 40960;
constexpr int TAB = 40960, CRED = 43008, KVT = 47104;
}

template <bool MEGA>
DEVI void phase1(Ctx& c, const u16* __restrict__ x, const u16* __restrict__ wqkv, const u16* __restrict__ wo,
                 const u16* __restrict__ wg, const u16* __restrict__ wu) {
    using namespace g1;
    STAMP(0, 0);
    LAS char* lds = c.lds;
    const int tid = c.tid, lane = c.lane, w = c.w;
    const int wm = w >> 1, wn = w & 1;
    const int xcd = c.bid & 7, loc = c.bid >> 3;
    const int pm = (xcd & 1) * 8 + (loc & 7);
    const int hp = (xcd >> 1) * 4 + (loc >> 3);
    const int s0 = (pm + hp) & 15;
    const int l15 = lane & 15, g = lane >> 4, q4 = (lane >> 2) & 3, p4 = lane & 3;

    unsigned voffA[2], voffB[3];
#pragma unroll
    for (int j = 0; j < 2; ++j) {
        const int row = 8 * (2 * w + j) + (lane >> 3), ch = (lane & 7) ^ ((row >> 1) & 7);
        voffA[j] = (unsigned)(row * DM * 2 + ch * 16 - j * 1024);
    }
#pragma unroll
    for (int j = 0; j < 3; ++j) {
        const int i = (3 * w + j) * 64 + lane;
        const int row = i / 24, cc = i % 24, blk = cc >> 3, cb = cc & 7;
        const int f = ((row >> 1) & 1) | (((row >> 3) & 1) << 1);
        voffB[j] = (unsigned)(row * (3 * DM) * 2 + (blk * DM + hp * 64 + (cb ^ (f << 1)) * 8) * 2 - j * 1024);
    }
    const char* gA = (const char*)x + (size_t)pm * 128 * DM * 2;
    const char* gB = (const char*)wqkv;
    const unsigned dA = c.lds0 + 2 * w * 1024, dB = c.lds0 + A_BYTES + 3 * w * 1024;
    auto issue = [&](int s, int j) {
        const int ck = (s0 + s) & 15;
        const unsigned buf = (unsigned)((s & 3) * STAGE);
        if (j == 0) glds<0>(gA + ck * 128, voffA[0], dA + buf);
        if (j == 1) glds<1024>(gA + ck * 128, voffA[1], dA + buf);
        if (j == 2) glds<0>(gB + (size_t)ck * (64 * 3 * DM * 2), voffB[0], dB + buf);
        if (j == 3) glds<1024>(gB + (size_t)ck * (64 * 3 * DM * 2), voffB[1], dB + buf);
        if (j == 4) glds<2048>(gB + (size_t)ck * (64 * 3 * DM * 2), voffB[2], dB + buf);
    };
#pragma unroll
    for (int s = 0; s < 4; ++s)
#pragma unroll
        for (int j = 0; j < 5; ++j) issue(s, j);
    const unsigned kvslot = WS_KVP + (hp * 16 + pm) * (2 * 1056 * 4);
    {
        const u32x4 mk = {SENT, SENT, SENT, SENT};
        st128(c.rs, kvslot + tid * 16, mk);
        st128(c.rs, kvslot + 8192 + (tid & 15) * 16, mk);
#pragma unroll
        for (int j = 0; j < 4; ++j) st128(c.rs, WS_X4 + c.bid * 32768 + j * 8192 + tid * 16, mk);
    }
    fence();

    const int fB = ((q4 >> 1) & 1) | ((g & 1) << 1);
    int va[4][2], vb[4][2];
#pragma unroll
    for (int s = 0; s < 4; ++s)
#pragma unroll
        for (int i = 0; i < 2; ++i) {
            va[s][i] = s * STAGE + (32 * wm + l15) * 128 + 16 * ((4 * i + g) ^ (l15 >> 1));
            vb[s][i] = s * STAGE + A_BYTES + (8 * g + q4) * B_PITCH + 32 * ((2 * wn + i) ^ fB) + 8 * p4;
        }
    f32x4 acc[2][6];
#pragma unroll
    for (int m = 0; m < 2; ++m)
#pragma unroll
        for (int n = 0; n < 6; ++n) acc[m][n] = (f32x4){0.f, 0.f, 0.f, 0.f};

    const unsigned floff = lane < 16 ? FL_F1R + pm * 128 + l15 * 8 : FL_F1C + hp * 128 + l15 * 8;
    const unsigned vlin = (unsigned)lane * 16u;
    auto issue_stats = [&](int j) {
        if (j < 2) {
            glds<0>(c.ws + WS_RS1 + pm * 16384 + (2 * w + j) * 1024, vlin, dA + j * 1024);
        } else {
            const int i = 3 * w + (j - 2);
            glds<0>(c.ws + WS_CS1 + hp * 12288 + (i < 12 ? i : 0) * 1024, vlin, dB + (j - 2) * 1024);
        }
    };
    const int cid = c.bid < 224 ? c.bid : c.bid - 32, cg = cid % 56, rr = cid / 56;
    const char* gC = (const char*)(cg < 28 ? wg : wu) + (size_t)(rr * 256 + w * 32) * (DF * 2) + (cg < 28 ? cg : cg - 28) * 256;
    const unsigned voffC = (unsigned)(g * (DF * 2) + l15 * 16);
    u32x4 cv[8];
    const RsGeom<false> gn(c, wo);

    LAS f32x2* rowtab = (LAS f32x2*)(lds + TAB);
    LAS float* coltab = (LAS float*)(lds + TAB + 1024);
    LAS unsigned* verdict = (LAS unsigned*)(lds + TAB + 1792);
    auto tables = [&]() {
        if (tid < 256) {
            const int row = tid >> 1, hf = tid & 1;
            f32x4 v[4];
#pragma unroll
            for (int i = 0; i < 4; ++i) {
                const f32x2 p0 = *(LAS f32x2*)(lds + ((8 * hf + 2 * i) * 128 + row) * 8);
                const f32x2 p1 = *(LAS f32x2*)(lds + ((8 * hf + 2 * i + 1) * 128 + row) * 8);
                v[i] = (f32x4){p0.x, p0.y, p1.x, p1.y};
            }
            float mean, m2;
            combine8(v, mean, m2);
            const float mo = __shfl_xor(mean, 1), m2o = __shfl_xor(m2, 1);
            const float d = mean - mo;
            const float var = (m2 + m2o + 256.f * d * d) * (1.f / DM);
            if (hf == 0) rowtab[row] = (f32x2){0.5f * (mean + mo), rsqrtf(var + LN_EPS)};
        } else if (tid < 448) {
            const int col = tid - 256;
            float s = 0.f;
#pragma unroll
            for (int i = 0; i < 16; ++i) s += *(LAS float*)(lds + A_BYTES + (i * 192 + col) * 4);
            coltab[col] = s;
        }
    };

    bf16x8 fa[2][2], fb[2][6];
    auto ldA = [&](int s, int kk, int m) { return lds_b128(lds + va[s][kk] + m * 2048); };
    auto ldB = [&](int s, int kk, int n) {
        LAS char* pb = lds + vb[s][n & 1] + kk * 32 * B_PITCH + (n >> 1) * 128;
        return lds_tr2(pb, pb + 4 * B_PITCH);
    };
    u32x2 fl;
    auto vm_slot = [&](auto t_tag, int k) {
        constexpr int T = decltype(t_tag)::value;
        if constexpr (T == 9) {
            if (k == 0) asm volatile("buffer_load_dwordx2 %0, %1, %2, 0 offen sc1" : "=v"(fl) : "v"(floff), "s"(c.rs) : "memory");
        }
        if constexpr (T >= 0 && T < 12) issue(T + 4, k);
        if constexpr (T == 12) issue_stats(k);
        if constexpr (T == 13) {
            if (k < 4) {
                asm volatile("global_load_dwordx4 %0, %1, %2" : "=v"(cv[2 * k]) : "v"(voffC), "s"(gC + (size_t)(2 * k) * (4 * DF * 2)) : "memory");
                asm volatile("global_load_dwordx4 %0, %1, %2" : "=v"(cv[2 * k + 1]) : "v"(voffC), "s"(gC + (size_t)(2 * k + 1) * (4 * DF * 2)) : "memory");
            } else if (MEGA) {
                gn.pieceB(c, 0); gn.pieceB(c, 1);
            }
        }
        if constexpr (T == 14 && MEGA) gn.pieceB(c, 2 + k);
        if constexpr (T == 15 && MEGA) { gn.pieceB(c, 7 + 2 * k); if (k < 2) gn.pieceB(c, 8 + 2 * k); }
    };
    auto half = [&](auto t_tag, auto kk_tag) {
        constexpr int t = decltype(t_tag)::value, kk = decltype(kk_tag)::value;
        constexpr int nt = kk ? t + 1 : t, nk = kk ^ 1, ns = nt & 3;
#pragma unroll
        for (int i = 0; i < 12; ++i) {
            const int m = i / 6, n = i % 6;
            if (n < 2) acc[m][n] = mfma16(fb[kk][n], fa[kk][m], acc[m][n]);
            else acc[m][n] = mfma16(fa[kk][m], fb[kk][n], acc[m][n]);
            if constexpr (nt < 16) {
                if (i == 0) { fa[nk][0] = ldA(ns, nk, 0); fa[nk][1] = ldA(ns, nk, 1); }
                else if (i <= 6) fb[nk][i - 1] = ldB(ns, nk, i - 1);
            }
            fence();
            if (kk == 1 && (i & 3) == 3) vm_slot(IC(t), i >> 2);
            if (kk == 0 && (i == 3 || i == 7)) vm_slot(IC(t - 1), 3 + (i >> 2));
            fence();
        }
    };
    auto step = [&](auto t_tag, auto wv_tag) {
        constexpr int t = decltype(t_tag)::value, WV = decltype(wv_tag)::value;
        half(t_tag, IC(0));
        if constexpr (WV >= 0) wait_vm<WV>();
        wait_lgkm0();
        fence();
        bar();
        fence();
        if constexpr (t == 3) {
            if (tid < 2) st64(c.rs, tid ? FL_F1C + hp * 128 + pm * 8 : FL_F1R + pm * 128 + hp * 8, c.tag);
            fence();
        }
        if constexpr (t == 13) {
            asm volatile("" : "+v"(fl));
            const bool ok = __all(fl.x == c.tag.x && fl.y == c.tag.y);
            if (lane == 0) verdict[w] = ok ? 0u : 1u;
            fence();
        }
        if constexpr (t == 14) { tables(); fence(); }
        half(t_tag, IC(1));
    };

    finish_tag<26>(c);
    wait_vm<21>();
    bar();
    STAMP(0, 1);
    fa[0][0] = ldA(0, 0, 0); fa[0][1] = ldA(0, 0, 1);
#pragma unroll
    for (int n = 0; n < 6; ++n) fb[0][n] = ldB(0, 0, n);
    {
        const int row = 16 * w + l15;
        const u32x4 r0 = __builtin_bit_cast(u32x4, lds_b128(lds + row * 128 + 16 * (g ^ (l15 >> 1))));
        const u32x4 r1 = __builtin_bit_cast(u32x4, lds_b128(lds + row * 128 + 16 * ((4 + g) ^ (l15 >> 1))));
        float v[16];
#pragma unroll
        for (int j = 0; j < 4; ++j) { v[2 * j] = bf_lo(r0[j]); v[2 * j + 1] = bf_hi(r0[j]); v[8 + 2 * j] = bf_lo(r1[j]); v[9 + 2 * j] = bf_hi(r1[j]); }
        float s = 0.f;
#pragma unroll
        for (int j = 0; j < 16; ++j) s += v[j];
        const float mu = xor16_32_sum(s) * (1.f / 64.f);
        float m2 = 0.f;
#pragma unroll
        for (int j = 0; j < 16; ++j) { const float d = v[j] - mu; m2 += d * d; }
        m2 = xor16_32_sum(m2);
        const int part = wm == 3 ? 0 : wm;
        f32x4 cs[2];
#pragma unroll
        for (int i = 0; i < 2; ++i) {
            cs[i] = (f32x4){0.f, 0.f, 0.f, 0.f};
#pragma unroll
            for (int kh = 0; kh < 2; ++kh) {
                LAS char* pb = lds + vb[0][i] + kh * 32 * B_PITCH + part * 128;
                cs[i] = mfma16(ones8(), lds_tr2(pb, pb + 4 * B_PITCH), cs[i]);
            }
        }
        st64(c.rs, WS_RS1 + ((pm * 16 + s0) * 128 + row) * 8, (u32x2){__float_as_uint(mu), __float_as_uint(m2)});
        st32(c.rs, WS_CS1 + ((hp * 16 + s0) * 192 + part * 64 + wn * 32 + (g & 1) * 16 + l15) * 4, __float_as_uint((g & 1) ? cs[1][0] : cs[0][0]));
    }
    fence();
    step(IC(0), IC(18)); step(IC(1), IC(18)); step(IC(2), IC(18)); step(IC(3), IC(10));
    step(IC(4), IC(10)); step(IC(5), IC(10)); step(IC(6), IC(10)); step(IC(7), IC(10));
    step(IC(8), IC(10)); step(IC(9), IC(10)); step(IC(10), IC(10)); step(IC(11), IC(10));
    step(IC(12), IC(10)); step(IC(13), IC(10)); step(IC(14), IC(MEGA ? 10 : 8)); step(IC(15), IC(-1));
    STAMP(0, 2);
    {
        const u32x4 v0 = *(LAS u32x4*)verdict, v1 = *(LAS u32x4*)(verdict + 4);
        if (__builtin_amdgcn_readfirstlane(umax4(v0) | umax4(v1))) {
            wait_flags(c.rs, floff, c.tag);
#pragma unroll
            for (int j = 0; j < 5; ++j) issue_stats(j);
            wait_vm<0>();
            bar();
            tables();
            wait_lgkm0();
            bar();
        }
    }
    const int rbase = 32 * wm;
    bf16x8 pk[2], pv[2];
    {
        f32x2 rt[2][4];
#pragma unroll
        for (int mm = 0; mm < 2; ++mm)
#pragma unroll
            for (int j = 0; j < 4; ++j) rt[mm][j] = rowtab[rbase + 16 * mm + 4 * g + j];
#pragma unroll
        for (int i = 0; i < 2; ++i) {
            const float cwk = coltab[64 + wn * 32 + i * 16 + l15], cwv = coltab[128 + wn * 32 + i * 16 + l15];
#pragma unroll
            for (int mm = 0; mm < 2; ++mm)
#pragma unroll
                for (int j = 0; j < 4; ++j) {
                    const float kk = round_bf16(rt[mm][j].y * (acc[mm][2 + i][j] - rt[mm][j].x * cwk));
                    const float vv = rt[mm][j].y * (acc[mm][4 + i][j] - rt[mm][j].x * cwv);
                    pk[i][mm * 4 + j] = (__bf16)phi_f(kk);
                    pv[i][mm * 4 + j] = (__bf16)vv;
                }
        }
    }
    LAS float* red = (LAS float*)lds;
    {
        LAS float* mine = red + w * 1056;
#pragma unroll
        for (int i = 0; i < 2; ++i) {
#pragma unroll
            for (int i2 = 0; i2 < 2; ++i2) {
                const f32x4 d = mfma16(pk[i], pv[i2], (f32x4){0.f, 0.f, 0.f, 0.f});
                *(LAS f32x4*)(mine + (16 * i2 + l15) * 32 + 16 * i + 4 * g) = d;
            }
            const f32x4 z = mfma16(pk[i], ones8(), (f32x4){0.f, 0.f, 0.f, 0.f});
            if (l15 == 0) *(LAS f32x4*)(mine + 1024 + 16 * i + 4 * g) = z;
        }
    }
    wait_lgkm0();
    bar();
    STAMP(0, 3);
    const int hh = tid >> 8, e4 = tid & 255;
    {
        f32x4 s = *(LAS f32x4*)(red + (hh + 0) * 1056 + e4 * 4);
        s += *(LAS f32x4*)(red + (hh + 2) * 1056 + e4 * 4);
        s += *(LAS f32x4*)(red + (hh + 4) * 1056 + e4 * 4);
        s += *(LAS f32x4*)(red + (hh + 6) * 1056 + e4 * 4);
        st128(c.rs, kvslot + tid * 16, no_sent(s));
        if (tid < 16) {
            const int h2 = tid >> 3, z4 = 1024 + (tid & 7) * 4;
            f32x4 z = *(LAS f32x4*)(red + (h2 + 0) * 1056 + z4);
            z += *(LAS f32x4*)(red + (h2 + 2) * 1056 + z4);
            z += *(LAS f32x4*)(red + (h2 + 4) * 1056 + z4);
            z += *(LAS f32x4*)(red + (h2 + 6) * 1056 + z4);
            st128(c.rs, kvslot + 8192 + tid * 16, no_sent(z));
        }
    }
    fence();
    bf16x8 pq[2];
#pragma unroll
    for (int mm = 0; mm < 2; ++mm) {
        const f32x2 rt = rowtab[rbase + 16 * mm + l15];
#pragma unroll
        for (int i = 0; i < 2; ++i) {
            const f32x4 cw = *(LAS f32x4*)(coltab + wn * 32 + i * 16 + 4 * g);
#pragma unroll
            for (int j = 0; j < 4; ++j) pq[mm][4 * i + j] = (__bf16)phi_f(round_bf16(rt.y * (acc[mm][i][j] - rt.x * cw[j])));
        }
    }
    {
        asm volatile("s_waitcnt vmcnt(%8)"
                     : "+v"(cv[0]), "+v"(cv[1]), "+v"(cv[2]), "+v"(cv[3]), "+v"(cv[4]), "+v"(cv[5]), "+v"(cv[6]), "+v"(cv[7])
                     : "n"(MEGA ? 13 : 1) : "memory");
        u32x4 sel;
#pragma unroll
        for (int j = 0; j < 4; ++j) sel[j] = (l15 >> 1) == j ? ((l15 & 1) ? 0x3f800000u : 0x00003f80u) : 0u;
        f32x4 d = {0.f, 0.f, 0.f, 0.f};
#pragma unroll
        for (int j = 0; j < 8; ++j) d = mfma16(__builtin_bit_cast(bf16x8, sel), __builtin_bit_cast(bf16x8, cv[j]), d);
        if (g < 2) *(LAS f32x4*)(lds + CRED + w * 512 + (8 * l15 + 4 * g) * 4) = d;
    }
    STAMP(0, 4);
    LAS u16* kvt = (LAS u16*)(lds + KVT);
    {
        const unsigned src = WS_KVP + hp * 16 * (2 * 1056 * 4);
        const u64 t0 = __builtin_amdgcn_s_memrealtime();
        for (;;) {
            const unsigned v = ld32(c.rs, src + l15 * (2 * 1056 * 4) + w * 1024);
            if (__all(v != SENT) || timed_out(t0)) break;
            __builtin_amdgcn_s_sleep(1);
        }
        STAMP(0, 5);
        auto gather = [&](auto z_tag) {
            constexpr bool Z = decltype(z_tag)::value;
            f32x4 s, z;
            for (;;) {
                u32x4 v[16], vz[4];
#pragma unroll
                for (int p = 0; p < 16; ++p) v[p] = ld128(c.rs, src + p * (2 * 1056 * 4) + tid * 16);
                if constexpr (Z) {
#pragma unroll
                    for (int p = 0; p < 4; ++p) vz[p] = ld128(c.rs, src + (4 * g + p) * (2 * 1056 * 4) + 8192 + l15 * 16);
                }
                s = (f32x4){0.f, 0.f, 0.f, 0.f}; z = s;
#pragma unroll
                for (int p = 0; p < 16; ++p) s += __builtin_bit_cast(f32x4, v[p]);
                if constexpr (Z) {
#pragma unroll
                    for (int p = 0; p < 4; ++p) z += __builtin_bit_cast(f32x4, vz[p]);
                }
                const f32x4 t = s + z;
                if (!__any(t[0] != t[0] || t[1] != t[1] || t[2] != t[2] || t[3] != t[3])) break;
                unsigned mx = 0;
#pragma unroll
                for (int p = 0; p < 16; ++p) mx = max(mx, umax4(v[p]));
                if constexpr (Z) {
#pragma unroll
                    for (int p = 0; p < 4; ++p) mx = max(mx, umax4(vz[p]));
                }
                if (!__any(mx == SENT) || timed_out(t0)) break;
            }
            *(LAS u32x2*)(kvt + hh * 1056 + e4 * 4) = (u32x2){pack_bf16(s[0], s[1]), pack_bf16(s[2], s[3])};
            if constexpr (Z) {
#pragma unroll
                for (int j = 0; j < 4; ++j) z[j] = xor16_32_sum(z[j]);
                if (lane < 16) *(LAS u32x2*)(kvt + (lane >> 3) * 1056 + 1024 + (lane & 7) * 4) = (u32x2){pack_bf16(z[0], z[1]), pack_bf16(z[2], z[3])};
            }
        };
        if (w == 1) gather(std::true_type{}); else gather(std::false_type{});
        wait_lgkm0();
        bar();
        STAMP(0, 6);
        LAS u16* kh = kvt + wn * 1056;
        const int e0 = 8 * (l15 >> 2) + (l15 & 3);
        auto rd8 = [&](LAS u16* p) {
            const u32x2 lo = *(LAS u32x2*)(p + 4 * g), hi = *(LAS u32x2*)(p + 16 + 4 * g);
            return __builtin_bit_cast(bf16x8, (u32x4){lo.x, lo.y, hi.x, hi.y});
        };
        const bf16x8 a0 = rd8(kh + e0 * 32), a1 = rd8(kh + (e0 + 4) * 32), az = rd8(kh + 1024);
        const f32x4 zero = {0.f, 0.f, 0.f, 0.f};
#pragma unroll
        for (int mm = 0; mm < 2; ++mm) {
            const f32x4 n0 = mfma16(a0, pq[mm], zero), n1 = mfma16(a1, pq[mm], zero), dn = mfma16(az, pq[mm], zero);
            const float inv = 1.f / (dn[0] + 1e-6f);
            u32x4 r;
            r.x = pack_bf16(n0[0] * inv, n0[1] * inv); r.y = pack_bf16(n0[2] * inv, n0[3] * inv);
            r.z = pack_bf16(n1[0] * inv, n1[1] * inv); r.w = pack_bf16(n1[2] * inv, n1[3] * inv);
            st128(c.rs, WS_O + ((pm * 128 + rbase + 16 * mm + l15) * DM + hp * 64 + wn * 32 + 8 * g) * 2, r);
        }
        if (tid >= 480) {
            const int q = tid - 480;
            f32x4 s = {0.f, 0.f, 0.f, 0.f};
#pragma unroll
            for (int j = 0; j < 8; ++j) s += *(LAS f32x4*)(lds + CRED + j * 512 + q * 16);
            st128(c.rs, WS_CS3 + (rr * 7168 + cg * 128 + q * 4) * 4, __builtin_bit_cast(u32x4, s));
        }
    }
    wait_vm<0>();
    bar();
    if (tid == 0) st64(c.rs, FL_F3 + pm * 128 + hp * 8, c.tag);
    STAMP(0, 7);
}

template <bool MEGA>
DEVI void phase2(const Ctx& c, const u16* __restrict__ wo, const u16* __restrict__ x,
                 const u16* __restrict__ wg, const u16* __restrict__ wu, const u16* __restrict__ wd) {
    using namespace g2;
    STAMP(2, 0);
    LAS char* lds = c.lds;
    const int tid = c.tid, lane = c.lane, w = c.w;
    const int wm = w >> 1, wn = w & 1;
    const int l15 = lane & 15, g = lane >> 4, q4 = (lane >> 2) & 3, p4 = lane & 3;
    const RsGeom<false> ge(c, wo);
    const int pm = ge.pm, pn = ge.pn;

    unsigned voffA[2];
#pragma unroll
    for (int j = 0; j < 2; ++j) {
        const int row = 8 * (2 * w + j) + (lane >> 3), ch = (lane & 7) ^ ((row >> 1) & 7);
        voffA[j] = (unsigned)(row * DM * 2 + ch * 16 - j * 1024);
    }
    const char* gA = c.ws + WS_O + (size_t)pm * 128 * DM * 2;
    const unsigned dA = c.lds0 + 2 * w * 1024;
    auto issueA = [&](int p, int j) {
        if (j == 0) glds<0>(gA + p * 128, voffA[0], dA + (p & 3) * 16384);
        else glds<1024>(gA + p * 128, voffA[1], dA + (p & 3) * 16384);
    };
    const int fB = ((q4 >> 1) & 1) | ((g & 1) << 1);
    int va[2], vb[2][2];
#pragma unroll
    for (int i = 0; i < 2; ++i) {
        va[i] = (32 * wm + l15) * 128 + 16 * ((4 * i + g) ^ (l15 >> 1));
        vb[0][i] = B_RING + (8 * g + q4) * 128 + 32 * ((2 * wn + i) ^ fB) + 8 * p4;
        vb[1][i] = vb[0][i] + 6 * B_PIECE;
    }
    const unsigned voffX = (unsigned)((tid >> 3) * DM * 2 + (tid & 7) * 16);
    const char* gX = (const char*)x + ((size_t)pm * 128 * DM + pn * 64) * 2;
    if constexpr (!MEGA) {
#pragma unroll
        for (int p = 0; p < 12; ++p) ge.pieceB(c, p);
        wait_vm<0>();
    }
    fence();
    if (w == 0) wait_flags(c.rs, FL_F3 + pm * 128 + l15 * 8, c.tag);
    bar();
    STAMP(2, 1);
#pragma unroll
    for (int p = 0; p < 4; ++p) { issueA(p, 0); issueA(p, 1); }
    u32x4 xr[2];
    asm volatile("global_load_dwordx4 %0, %1, %2" : "=v"(xr[0]) : "v"(voffX), "s"(gX) : "memory");
    asm volatile("global_load_dwordx4 %0, %1, %2" : "=v"(xr[1]) : "v"(voffX), "s"(gX + 64 * DM * 2) : "memory");

    f32x4 acc[2][2];
#pragma unroll
    for (int m = 0; m < 2; ++m)
#pragma unroll
        for (int n = 0; n < 2; ++n) acc[m][n] = (f32x4){0.f, 0.f, 0.f, 0.f};
    bf16x8 fa[2][2], fb[2][2];
    auto ldA = [&](int p, int kk, int m) { return lds_b128(lds + va[kk] + (p & 3) * 16384 + m * 2048); };
    auto ldB = [&](int p, int kk, int n) {
        const int s = p % 12;
        LAS char* pb = lds + vb[s / 6][n] + (s % 6) * B_PIECE + kk * 4096;
        return lds_tr2(pb, pb + 512);
    };
    auto half = [&](auto t_tag, auto kk_tag) {
        constexpr int t = decltype(t_tag)::value, kk = decltype(kk_tag)::value;
        constexpr int nt = kk ? t + 1 : t, nk = kk ^ 1;
#pragma unroll
        for (int i = 0; i < 4; ++i) {
            const int m = i >> 1, n = i & 1;
            if constexpr (nt < 16) {
                if (i == 0) { fa[nk][0] = ldA(nt, nk, 0); fa[nk][1] = ldA(nt, nk, 1); fb[nk][0] = ldB(nt, nk, 0); }
                if (i == 1) fb[nk][1] = ldB(nt, nk, 1);
            }
            acc[m][n] = mfma16(fb[kk][n], fa[kk][m], acc[m][n]);
            fence();
            if constexpr (kk == 1 && t + 4 < 16) {
                if (i == 1 || i == 2) issueA(t + 4, i - 1);
                if (i == 3 && t < 4) ge.pieceB(c, 12 + t);
            }
            fence();
        }
    };
    auto step = [&](auto t_tag, auto wv_tag) {
        constexpr int WV = decltype(wv_tag)::value;
        half(t_tag, IC(0));
        if constexpr (WV >= 0) {
            wait_vm<WV>();
            wait_lgkm0();
            fence();
            bar();
            fence();
        }
        half(t_tag, IC(1));
    };
    wait_vm<8>();
    bar();
    fa[0][0] = ldA(0, 0, 0); fa[0][1] = ldA(0, 0, 1); fb[0][0] = ldB(0, 0, 0); fb[0][1] = ldB(0, 0, 1);
    fence();
    step(IC(0), IC(6)); step(IC(1), IC(7)); step(IC(2), IC(8)); step(IC(3), IC(7));
    step(IC(4), IC(7)); step(IC(5), IC(6)); step(IC(6), IC(5)); step(IC(7), IC(4));
    step(IC(8), IC(4)); step(IC(9), IC(4)); step(IC(10), IC(4)); step(IC(11), IC(4));
    step(IC(12), IC(4)); step(IC(13), IC(2)); step(IC(14), IC(0)); step(IC(15), IC(-1));
    STAMP(2, 2);
    asm volatile("" : "+v"(xr[0]), "+v"(xr[1]));

    constexpr int OP = 144;
#pragma unroll
    for (int m = 0; m < 2; ++m)
#pragma unroll
        for (int n = 0; n < 2; ++n)
            *(LAS u32x2*)(lds + (32 * wm + 16 * m + l15) * OP + (32 * wn + 16 * n + 4 * g) * 2) =
                (u32x2){pack_bf16(acc[m][n][0], acc[m][n][1]), pack_bf16(acc[m][n][2], acc[m][n][3])};
    wait_lgkm0();
    bar();
    STAMP(2, 3);
#pragma unroll
    for (int i = 0; i < 2; ++i) {
        const int r = 64 * i + (tid >> 3), row = pm * 128 + r;
        const u32x4 av = *(LAS u32x4*)(lds + r * OP + (tid & 7) * 16);
        float hv[8];
#pragma unroll
        for (int j = 0; j < 4; ++j) {
            hv[2 * j] = round_bf16(bf_lo(xr[i][j]) + bf_lo(av[j]));
            hv[2 * j + 1] = round_bf16(bf_hi(xr[i][j]) + bf_hi(av[j]));
        }
        st128(c.rs, WS_H + (row * DM + pn * 64 + (tid & 7) * 8) * 2,
              (u32x4){pack_bf16(hv[0], hv[1]), pack_bf16(hv[2], hv[3]), pack_bf16(hv[4], hv[5]), pack_bf16(hv[6], hv[7])});
        float s = ((hv[0] + hv[1]) + (hv[2] + hv[3])) + ((hv[4] + hv[5]) + (hv[6] + hv[7]));
        s += __shfl_xor(s, 1); s += __shfl_xor(s, 2); s += __shfl_xor(s, 4);
        const float mu = s * (1.f / 64.f);
        float m2 = 0.f;
#pragma unroll
        for (int j = 0; j < 8; ++j) { const float d = hv[j] - mu; m2 += d * d; }
        m2 += __shfl_xor(m2, 1); m2 += __shfl_xor(m2, 2); m2 += __shfl_xor(m2, 4);
        if ((tid & 7) == 0) st64(c.rs, WS_ST2 + (row * 16 + pn) * 8, (u32x2){__float_as_uint(mu), __float_as_uint(m2)});
    }
    STAMP(2, 4);
    if (MEGA && w != 0) {
        const G3Geom gn(c, wg, wu, wd);
        gn.prefetchB(c);
        wait_vm<8>();
    } else {
        wait_vm<0>();
    }
    bar();
    if (tid == 0) st64(c.rs, FL_F4 + pm * 128 + pn * 8, c.tag);
    STAMP(2, 7);
}

template <bool MEGA>
DEVI void phase4(const Ctx& c, const u16* __restrict__ wd, u16* __restrict__ out) {
    using namespace g4;
    STAMP(4, 0);
    LAS char* lds = c.lds;
    const int tid = c.tid, lane = c.lane, w = c.w;
    const int wm = w >> 1, wn = w & 1;
    const int l15 = lane & 15, g = lane >> 4, q4 = (lane >> 2) & 3, p4 = lane & 3;
    const RsGeom<true> ge(c, wd);
    const int pm = ge.pm, pn = ge.pn, ks = ge.ks;

    unsigned voffA[2];
#pragma unroll
    for (int j = 0; j < 2; ++j) {
        const int row = 8 * (2 * w + j) + (lane >> 3), ch = (lane & 7) ^ ((row >> 1) & 7);
        voffA[j] = (unsigned)(row * DF * 2 + ch * 16 - j * 1024);
    }
    const char* gA = c.ws + WS_HH + (size_t)pm * 128 * DF * 2 + ks * 3584;
    const unsigned dA = c.lds0 + 2 * w * 1024, dB = c.lds0 + B_RING + 2 * w * 1024;
    int va[2][2], vb[4];
#pragma unroll
    for (int kk = 0; kk < 2; ++kk)
#pragma unroll
        for (int m = 0; m < 2; ++m) va[kk][m] = (32 * wm + 16 * (m ^ ks) + l15) * 128 + 16 * ((4 * kk + g) ^ (l15 >> 1));
    {
        const int sB = q4 | ((g & 1) << 2);
#pragma unroll
        for (int n = 0; n < 4; ++n) vb[n] = B_RING + (8 * g + q4) * 256 + 32 * ((4 * wn + n) ^ sB) + 8 * p4;
    }
    if constexpr (!MEGA) {
#pragma unroll
        for (int p = 0; p < 4; ++p) ge.pieceB(c, p);
        wait_vm<0>();
    }
    fence();
    if (w == 0) wait_flags(c.rs, FL_F6 + (pm >> 1) * 256 + ks * 128 + l15 * 8, c.tag);
    bar();
    STAMP(4, 1);
#pragma unroll
    for (int p = 0; p < 4; ++p) { glds<0>(gA + p * 128, voffA[0], dA + p * 16384); glds<1024>(gA + p * 128, voffA[1], dA + p * 16384); }

    f32x4 acc[2][4];
#pragma unroll
    for (int m = 0; m < 2; ++m)
#pragma unroll
        for (int n = 0; n < 4; ++n) acc[m][n] = (f32x4){0.f, 0.f, 0.f, 0.f};
    bf16x8 fa[2][2], fb[2][4];
    auto ldA = [&](int s, int kk, int m) { return lds_b128(lds + va[kk][m] + s * 16384); };
    auto ldB = [&](int s, int kk, int n) {
        LAS char* pb = lds + vb[n] + s * B_PIECE + kk * 8192;
        return lds_tr2(pb, pb + 1024);
    };
    u32x4 hr[2];
    const unsigned voffH = WS_H + (unsigned)(((pm * 128 + 32 * (tid >> 8) + 16 * ks + ((tid >> 4) & 15)) * DM + pn * 128 + (tid & 15) * 8) * 2);
    const char* pa = gA + 4 * 128;
    const char* pb = ge.gB + (size_t)4 * (64 * 2048);
    auto half = [&](auto s_tag, auto kk_tag, auto iss_tag, auto last_tag) {
        constexpr int s = decltype(s_tag)::value, kk = decltype(kk_tag)::value, ISS = decltype(iss_tag)::value;
        constexpr bool LAST = decltype(last_tag)::value;
        constexpr int ns = kk ? (s + 1) & 3 : s, nk = kk ^ 1;
#pragma unroll
        for (int i = 0; i < 8; ++i) {
            const int m = i >> 2, n = i & 3;
            acc[m][n] = mfma16(fb[kk][n], fa[kk][m], acc[m][n]);
            if constexpr (!LAST) {
                if (i == 0) { fa[nk][0] = ldA(ns, nk, 0); fa[nk][1] = ldA(ns, nk, 1); }
                else if (i <= 4) fb[nk][i - 1] = ldB(ns, nk, i - 1);
            }
            fence();
            if constexpr (kk == 1 && ISS == 1) {
                if (i == 1) glds<0>(pa + s * 128, voffA[0], dA + s * 16384);
                if (i == 3) glds<1024>(pa + s * 128, voffA[1], dA + s * 16384);
                if (i == 5) glds<0>(pb + (size_t)s * (64 * 2048), ge.voffB[0], dB + s * B_PIECE);
                if (i == 7) glds<1024>(pb + (size_t)s * (64 * 2048), ge.voffB[1], dB + s * B_PIECE);
            }
            if constexpr (kk == 1 && ISS == 2) {
                if (i == 1) asm volatile("buffer_load_dwordx4 %0, %1, %2, 0 offen sc1" : "=v"(hr[0]) : "v"(voffH), "s"(c.rs) : "memory");
                if (i == 3) asm volatile("buffer_load_dwordx4 %0, %1, %2, %3 offen sc1" : "=v"(hr[1]) : "v"(voffH), "s"(c.rs), "s"(64 * DM * 2) : "memory");
            }
            fence();
        }
    };
    auto step = [&](auto s_tag, auto iss_tag, auto wv_tag) {
        constexpr int WV = decltype(wv_tag)::value;
        half(s_tag, IC(0), iss_tag, std::false_type{});
        if constexpr (WV >= 0) {
            wait_vm<WV>();
            wait_lgkm0();
            fence();
            bar();
            fence();
        }
        half(s_tag, IC(1), iss_tag, std::integral_constant<bool, (WV < 0)>{});
    };
    wait_vm<6>();
    bar();
    fa[0][0] = ldA(0, 0, 0); fa[0][1] = ldA(0, 0, 1);
#pragma unroll
    for (int n = 0; n < 4; ++n) fb[0][n] = ldB(0, 0, n);
    fence();
    step(IC(0), IC(1), IC(4)); step(IC(1), IC(1), IC(6)); step(IC(2), IC(1), IC(8)); step(IC(3), IC(1), IC(8));
    for (int it = 0; it < 5; ++it) {
        pa += 4 * 128; pb += (size_t)4 * (64 * 2048);
        step(IC(0), IC(1), IC(8)); step(IC(1), IC(1), IC(8)); step(IC(2), IC(1), IC(8)); step(IC(3), IC(1), IC(8));
    }
    step(IC(0), IC(0), IC(8)); step(IC(1), IC(0), IC(4)); step(IC(2), IC(2), IC(0)); step(IC(3), IC(0), IC(-1));
    STAMP(4, 2);

    constexpr int OP = 272;
    const unsigned xs = WS_X4 + ((c.bid ^ 1) * 8 + w) * 4096 + lane * 16;
#pragma unroll
    for (int n = 0; n < 4; ++n) st128(c.rs, xs + n * 1024, no_sent(acc[1][n]));
    const unsigned xr = WS_X4 + (c.bid * 8 + w) * 4096 + lane * 16;
    u32x4 pv[4];
    {
        const u64 t0 = __builtin_amdgcn_s_memrealtime();
        for (;;) {
#pragma unroll
            for (int n = 0; n < 4; ++n) pv[n] = ld128(c.rs, xr + n * 1024);
            const unsigned mx = max(max(umax4(pv[0]), umax4(pv[1])), max(umax4(pv[2]), umax4(pv[3])));
            if (!__any(mx == SENT) || timed_out(t0)) break;
            __builtin_amdgcn_s_sleep(1);
        }
    }
    STAMP(4, 3);
    asm volatile("" : "+v"(hr[0]), "+v"(hr[1]));
#pragma unroll
    for (int n = 0; n < 4; ++n) {
        const f32x4 s = acc[0][n] + __builtin_bit_cast(f32x4, pv[n]);
        *(LAS u32x2*)(lds + (16 * wm + l15) * OP + (64 * wn + 16 * n + 4 * g) * 2) = (u32x2){pack_bf16(s[0], s[1]), pack_bf16(s[2], s[3])};
    }
    wait_lgkm0();
    bar();
    STAMP(4, 4);
#pragma unroll
    for (int i = 0; i < 2; ++i) {
        const int r = 32 * i + (tid >> 4), trow = 32 * (r >> 4) + 16 * ks + (r & 15);
        const u32x4 av = *(LAS u32x4*)(lds + r * OP + (tid & 15) * 16);
        u32x4 o;
#pragma unroll
        for (int j = 0; j < 4; ++j) o[j] = pack_bf16(bf_lo(hr[i][j]) + bf_lo(av[j]), bf_hi(hr[i][j]) + bf_hi(av[j]));
        *(u32x4*)(out + (size_t)(pm * 128 + trow) * DM + pn * 128 + (tid & 15) * 8) = o;
    }
    STAMP(4, 7);
}

template <bool MEGA>
DEVI void phase3(const Ctx& c, const u16* __restrict__ wg, const u16* __restrict__ wu, const u16* __restrict__ wd) {
    using namespace g3;
    STAMP(3, 0);
    LAS char* lds = c.lds;
    const int tid = c.tid, lane = c.lane, w = c.w;
    const int grp = w >> 2, wm = w >> 1, wn = w & 1;
    const int l15 = lane & 15, g = lane >> 4, q4 = (lane >> 2) & 3, p4 = lane & 3;
    const G3Geom ge(c, wg, wu, wd);
    const int pm = ge.pm, pn = ge.pn;

    unsigned voffA[2];
#pragma unroll
    for (int j = 0; j < 2; ++j) {
        const int row = 8 * (2 * w + j) + (lane >> 3), ch = (lane & 7) ^ ((row >> 1) & 7);
        voffA[j] = (unsigned)(row * DM * 2 + ch * 16 - j * 1024);
    }
    const char* gA = c.ws + WS_H + (size_t)pm * 256 * DM * 2;
    const unsigned ldsA = c.lds0 + 2 * w * 1024;

    if (w == 0) {
        const int cgp = (pn * 112 + (lane & 1) * 111) / 128 + ((lane >> 1) & 1) * 28, pb = ((lane >> 2) & 3) * 56 + cgp;
        const int ppm = (pb & 1) * 8 + ((pb >> 3) & 7), php = ((pb & 7) >> 1) * 4 + (pb >> 6);
        wait_flags(c.rs, lane < 32 ? FL_F4 + pm * 256 + lane * 8 : FL_F3 + ppm * 128 + php * 8, c.tag);
    }
    if (w == 0 || !MEGA) ge.prefetchB(c);
    bar();
    STAMP(3, 1);
    auto issueA = [&](int p, int slot) {
        const char* a = gA + (size_t)(p & 1) * AHALF + (p >> 1) * 128;
        glds<0>(a, voffA[0], ldsA + slot * A_PIECE); glds<1024>(a, voffA[1], ldsA + slot * A_PIECE);
    };
    issueA(0, 0); issueA(1, 1);
    f32x4 sv[4];
    asm_load4(c.rs, WS_ST2 + pm * 256 * 128 + tid * 64, sv);
    float cp[4];
    {
        const int t2 = min(tid, 223);
        const unsigned o = WS_CS3 + (unsigned)(pn * 112 + t2 + (t2 >= 112 ? DF - 112 : 0)) * 4;
        asm volatile("buffer_load_dword %0, %4, %5, 0 offen sc1\n\tbuffer_load_dword %1, %4, %5, %6 offen sc1\n\t"
                     "buffer_load_dword %2, %4, %5, %7 offen sc1\n\tbuffer_load_dword %3, %4, %5, %8 offen sc1"
                     : "=&v"(cp[0]), "=&v"(cp[1]), "=&v"(cp[2]), "=&v"(cp[3])
                     : "v"(o), "s"(c.rs), "s"(7168 * 4), "s"(2 * 7168 * 4), "s"(3 * 7168 * 4)
                     : "memory");
    }
    issueA(2, 2);
    if (grp == 1) issueA(3, 3);
    LAS f32x4* rstat = (LAS f32x4*)(lds + TAB_OFF);
    LAS float* ctab = (LAS float*)(lds + TAB_OFF + 4096);
    auto row_table = [&]() {
        float mean, m2;
        combine8(sv, mean, m2);
        const float mo = __shfl_xor(mean, 1), m2o = __shfl_xor(m2, 1);
        const float d = mean - mo;
        const float ve = (m2 + m2o + 256.f * d * d) * (1.f / DM) + LN_EPS;
        const float r = rsqrtf(ve);
        const float c1 = 2.3022081985f, c2 = 0.1029432397f;
        if ((tid & 1) == 0) rstat[tid >> 1] = (f32x4){0.5f * (mean + mo), -c1 * r, -c2 * r * r * r, ve};
        asm volatile("" : "+v"(cp[0]), "+v"(cp[1]), "+v"(cp[2]), "+v"(cp[3]));
        if (tid < 224) ctab[tid] = (cp[0] + cp[1]) + (cp[2] + cp[3]);
    };

    int aoff[2];
#pragma unroll
    for (int ks = 0; ks < 2; ++ks) aoff[ks] = (64 * (wm & 1) + l15) * 128 + 16 * ((4 * ks + g) ^ (l15 >> 1));
    const int brow = (16 * (g >> 1) + 4 * (g & 1) + q4) * 224;
    const int boff = B_OFF + brow + 112 * wn + 8 * p4;
    const int boffX = B_OFF + brow + 112 * wn + 96 + (p4 < 2 ? 8 * p4 : B_SUB + 8 * (p4 - 2));

    f32x4 acc[4][7];
#pragma unroll
    for (int m = 0; m < 4; ++m)
#pragma unroll
        for (int n = 0; n < 7; ++n) acc[m][n] = (f32x4){0.f, 0.f, 0.f, 0.f};

    unsigned voffAe[2], voffAo[2], voffBo[2];
#pragma unroll
    for (int j = 0; j < 2; ++j) {
        voffAe[j] = voffA[j] + (grp ? 256u : AHALF + 128u);
        voffAo[j] = voffA[j] + (grp ? AHALF + 256u : 256u);
        voffBo[j] = ge.voffB[j] + BSTEP;
    }
    const char* pA = gA;
    const char* pB0 = ge.gB[0] + (size_t)4 * BSTEP;
    const char* pB1 = ge.gB[1] + (size_t)4 * BSTEP;
    int rdB = 0, prevB = 4 * B_PIECE;
    int rdA = grp * A_PIECE;
    int vB = boff, vBX = boffX, vA0 = aoff[0] + rdA, vA1 = aoff[1] + rdA;
    auto ringA = [&](int xx, int k) { xx += k * A_PIECE; return xx >= NR * A_PIECE ? xx - NR * A_PIECE : xx; };

    auto unit = [&](auto ks_tag, auto iss_tag, auto wv_tag) {
        constexpr int ks = decltype(ks_tag)::value, ISS = decltype(iss_tag)::value, WV = decltype(wv_tag)::value;
        bf16x8 a[4], b[7];
#pragma unroll
        for (int n = 0; n < 6; ++n) {
            LAS char* pb = lds + vB + (n >= 3 ? B_SUB : 0) + (n % 3) * 32;
            b[n] = lds_tr2(pb, pb + 8 * 224);
        }
        { LAS char* pb = lds + vBX; b[6] = lds_tr2(pb, pb + 8 * 224); }
#pragma unroll
        for (int m = 0; m < 4; ++m) a[m] = lds_b128(lds + (ks ? vA1 : vA0) + m * 2048);
        fence();
        const unsigned dA = ldsA + ringA(rdA, 3 + ks), dB = ge.ldsB + ((unsigned)prevB & ge.ringmask);
        const char* qA = pA; const char* qB0 = pB0; const char* qB1 = pB1;
        if constexpr (ISS == 1) {
            if constexpr (ks == 1) { pA += 128; pB0 += 2 * BSTEP; pB1 += 2 * BSTEP; }
        } else if constexpr (ISS == 2) {
            if (grp == 0) { glds<0>(qA, voffAe[0], dA); glds<1024>(qA, voffAe[1], dA); }
        }
        prevB = rdB;
        rdB = rdB == (NR - 1) * B_PIECE ? 0 : rdB + B_PIECE;
        vB = boff + rdB; vBX = boffX + rdB;
        if constexpr (ks == 1) { rdA = ringA(rdA, 2); vA0 = aoff[0] + rdA; vA1 = aoff[1] + rdA; }
        fence();
        if constexpr (WV >= 0) wait_vm<WV>();
        wait_lgkm0();
        fence();
        bar();
        fence();
        __builtin_amdgcn_s_setprio(1);
#pragma unroll
        for (int m = 0; m < 4; ++m) {
#pragma unroll
            for (int n = 0; n < 7; ++n) {
                acc[m][n] = mfma16(b[n], a[m], acc[m][n]);
                if constexpr (ISS == 1) {
                    if (n == 3) {
                        fence();
                        if (m == 0) glds<0>(qA, ks ? voffAo[0] : voffAe[0], dA);
                        if (m == 1) glds<1024>(qA, ks ? voffAo[1] : voffAe[1], dA);
                        if (m == 2) glds<0>(qB0, ks ? voffBo[0] : ge.voffB[0], dB);
                        if (m == 3) glds<1024>(qB1, ks ? voffBo[1] : ge.voffB[1], dB);
                        fence();
                    }
                }
            }
        }
        __builtin_amdgcn_s_setprio(0);
        fence();
        bar();
        fence();
    };
    wait_vm<12>();
    bar();
    if (grp == 1) bar();
    unit(IC(0), IC(1), IC(2));
    wait_regs4<6>(sv);
    row_table();
    unit(IC(1), IC(1), IC(4));
    for (int t = 1; t < 14; ++t) {
        unit(IC(0), IC(1), IC(6));
        unit(IC(1), IC(1), IC(6));
    }
    unit(IC(0), IC(2), IC(8));
    unit(IC(1), IC(0), IC(2));
    unit(IC(0), IC(0), IC(0));
    unit(IC(1), IC(0), IC(-1));
    if (grp == 0) bar();
    STAMP(3, 2);
    if constexpr (MEGA) {
        const RsGeom<true> gn(c, wd);
#pragma unroll
        for (int p = 0; p < 4; ++p) gn.pieceB(c, p);
    }
    f32x4 cw[7];
#pragma unroll
    for (int n = 0; n < 6; ++n) cw[n] = *(LAS f32x4*)(ctab + (n / 3) * 112 + wn * 56 + (n % 3) * 16 + 4 * g);
    cw[6] = *(LAS f32x4*)(ctab + (g >> 1) * 112 + wn * 56 + 48 + 4 * (g & 1));
    f32x4 st[4];
#pragma unroll
    for (int m = 0; m < 4; ++m) st[m] = rstat[64 * wm + 16 * m + l15];
    auto geglu2 = [](f32x2 G, f32x2 U, const f32x4& s) {
        const f32x2 a = G * (G * G * s[2] + s[1]);
        f32x2 e; e.x = __builtin_amdgcn_exp2f(a.x); e.y = __builtin_amdgcn_exp2f(a.y);
        const f32x2 d = e * s[3] + s[3];
        f32x2 r; r.x = __builtin_amdgcn_rcpf(d.x); r.y = __builtin_amdgcn_rcpf(d.y);
        const f32x2 o = (G * U) * r;
        return pack_bf16(o.x, o.y);
    };
    LAS char* orow = lds + (64 * wm + l15) * OPITCH + 112 * wn + 8 * g;
    const unsigned gdst = WS_HH + (pm * 256 * DF + pn * 112) * 2;
#pragma unroll
    for (int mp = 0; mp < 2; ++mp) {
#pragma unroll
        for (int m = 2 * mp; m < 2 * mp + 2; ++m) {
#pragma unroll
            for (int n = 0; n < 3; ++n) {
                const f32x4 gg = acc[m][n] - st[m][0] * cw[n];
                const f32x4 uu = acc[m][n + 3] - st[m][0] * cw[n + 3];
                u32x2 o;
                o.x = geglu2(gg.xy, uu.xy, st[m]);
                o.y = geglu2(gg.zw, uu.zw, st[m]);
                *(LAS u32x2*)(orow + m * 16 * OPITCH + 32 * n) = o;
            }
        }
        {
            const f32x4 y0 = acc[2 * mp][6] - st[2 * mp][0] * cw[6];
            const f32x4 y1 = acc[2 * mp + 1][6] - st[2 * mp + 1][0] * cw[6];
            const f32x4 sx = lane < 32 ? st[2 * mp] : st[2 * mp + 1];
            f32x4 gg, uu;
#pragma unroll
            for (int j = 0; j < 4; ++j) {
                auto r = __builtin_amdgcn_permlane32_swap(__float_as_uint(y0[j]), __float_as_uint(y1[j]), false, false);
                gg[j] = __uint_as_float(r[0]); uu[j] = __uint_as_float(r[1]);
            }
            *(LAS u32x2*)(lds + (64 * wm + 16 * (2 * mp + (lane >> 5)) + l15) * OPITCH + 112 * wn + 96 + 8 * (g & 1)) =
                (u32x2){geglu2(gg.xy, uu.xy, sx), geglu2(gg.zw, uu.zw, sx)};
        }
        wait_lgkm0();
        fence();
        bar();
        fence();
        STAMP(3, 3 + mp);
        int r = tid / 14, cc = tid % 14;
#pragma unroll
        for (int j = 0; j < 4; ++j) {
            if (j < 3 || tid < 256) {
                const int row = 64 * (r >> 5) + 32 * mp + (r & 31);
                const u32x4 v = *(LAS u32x4*)(lds + row * OPITCH + cc * 16);
                st128(c.rs, gdst + (unsigned)(row * (DF * 2) + cc * 16), v);
            }
            r += 36; cc += 8;
            if (cc >= 14) { cc -= 14; r += 1; }
        }
        fence();
    }
    STAMP(3, 5);
    wait_vm<0>();
    bar();
    if (tid == 0) st64(c.rs, FL_F6 + pm * 256 + pn * 8, c.tag);
    STAMP(3, 7);
}

template <int PHASES>
__global__ __launch_bounds__(512, 2) void fused_kernel(const u16* __restrict__ x, const u16* __restrict__ wqkv,
                                                       const u16* __restrict__ wo, const u16* __restrict__ wg,
                                                       const u16* __restrict__ wu, const u16* __restrict__ wd,
                                                       u16* __restrict__ out, char* __restrict__ ws) {
    constexpr bool MEGA = PHASES == 15;
    extern __shared__ __attribute__((aligned(16))) char smem_raw[];
    Ctx c;
    c.lds = (LAS char*)smem_raw;
    c.lds0 = (unsigned)(uintptr_t)c.lds;
    c.rs = __builtin_amdgcn_make_buffer_rsrc((void*)ws, 0, WS_END, 0x00020000);
    c.ws = ws;
    c.tid = threadIdx.x; c.lane = c.tid & 63;
    c.w = __builtin_amdgcn_readfirstlane(c.tid >> 6);
    c.bid = blockIdx.x;
    {
        asm volatile("buffer_load_dwordx2 %0, off, %1, 0 sc1" : "=v"(c.tag) : "s"(c.rs) : "memory");
        if constexpr (!(PHASES & 1)) finish_tag<0>(c);
    }
    if constexpr (PHASES & 1) phase1<MEGA>(c, x, wqkv, wo, wg, wu);
    if constexpr (PHASES & 2) phase2<MEGA>(c, wo, x, wg, wu, wd);
    if constexpr (PHASES & 4) phase3<MEGA>(c, wg, wu, wd);
    if constexpr (PHASES & 8) {
        phase4<MEGA>(c, wd, out);
        if (c.bid == 0 && c.tid == 0) st64(c.rs, FL_EPOCH, c.tag);
    }
}

template <int PHASES>
static void launch_phases(void* const* d_in, void* d_out, void* d_ws, hipStream_t stream) {
    (void)hipFuncSetAttribute((const void*)fused_kernel<PHASES>, hipFuncAttributeMaxDynamicSharedMemorySize, LDS_BYTES);
    fused_kernel<PHASES><<<256, 512, LDS_BYTES, stream>>>((const u16*)d_in[0], (const u16*)d_in[1], (const u16*)d_in[2],
                                                          (const u16*)d_in[4], (const u16*)d_in[5], (const u16*)d_in[6],
                                                          (u16*)d_out, (char*)d_ws);
}

extern "C" void kernel_launch(void* const* d_in, const int* in_sizes, int n_in, void* d_out, int out_size,
                              void* d_ws, size_t ws_size, hipStream_t stream) {
    launch_phases<15>(d_in, d_out, d_ws, stream);
}
```

```cpp
#include <hip/hip_runtime.h>
#include <hip/hip_bf16.h>
#include <stdint.h>

#define LAS __attribute__((address_space(3)))
#define GAS __attribute__((address_space(1)))
#define DEVI __device__ __forceinline__

typedef __attribute__((ext_vector_type(8))) __bf16 bf16x8;
typedef __attribute__((ext_vector_type(4))) __bf16 bf16x4;
typedef __attribute__((ext_vector_type(2))) __bf16 bf16x2;
typedef __attribute__((ext_vector_type(4))) short s16x4;
typedef __attribute__((ext_vector_type(8))) short s16x8;
typedef __attribute__((ext_vector_type(4))) float f32x4;
typedef __attribute__((ext_vector_type(2))) float f32x2;
typedef __attribute__((ext_vector_type(4))) unsigned u32x4;
typedef __attribute__((ext_vector_type(2))) unsigned u32x2;
typedef unsigned short u16;

constexpr int SEQ = 2048, DM = 1024, NH = 32, DH = 32, DF = 3584;
constexpr float LN_EPS = 1e-5f;

DEVI unsigned pack_bf16(float lo, float hi) {
    bf16x2 v; v.x = (__bf16)lo; v.y = (__bf16)hi;
    return __builtin_bit_cast(unsigned, v);
}
DEVI float bf_lo(unsigned u) { return __uint_as_float(u << 16); }
DEVI float bf_hi(unsigned u) { return __uint_as_float(u & 0xffff0000u); }
DEVI float round_bf16(float x) { return (float)(__bf16)x; }

DEVI void glds16(const char* sbase, unsigned voff, unsigned lds_dst) {
    asm volatile("s_mov_b32 m0, %2\n\ts_nop 0\n\tglobal_load_lds_dwordx4 %0, %1"
                 :
                 : "v"(voff), "s"(sbase), "s"(lds_dst)
                 : "memory");
}
template <int OFF> DEVI void glds16o(const char* sbase, unsigned voff, unsigned lds_dst) {
    asm volatile("s_mov_b32 m0, %2\n\ts_nop 0\n\tglobal_load_lds_dwordx4 %0, %1 offset:%3"
                 :
                 : "v"(voff), "s"(sbase), "s"(lds_dst), "n"(OFF)
                 : "memory");
}
#define G3VAR 0
DEVI void glds16x2(const char* sbase0, unsigned voff0, const char* sbase1, unsigned voff1, unsigned lds_dst) {
    asm volatile("s_mov_b32 m0, %4\n\ts_nop 0\n\tglobal_load_lds_dwordx4 %0, %1\n\tglobal_load_lds_dwordx4 %2, %3 offset:1024"
                 :
                 : "v"(voff0), "s"(sbase0), "v"(voff1), "s"(sbase1), "s"(lds_dst)
                 : "memory");
}
DEVI bf16x8 lds_read_b128(LAS char* p) {
    return *(LAS bf16x8*)p;
}
DEVI bf16x8 lds_read_tr2(LAS char* p0, LAS char* p1) {
    s16x4 a = __builtin_amdgcn_ds_read_tr16_b64_v4i16((LAS s16x4*)p0);
    s16x4 b = __builtin_amdgcn_ds_read_tr16_b64_v4i16((LAS s16x4*)p1);
    s16x8 r = __builtin_shufflevector(a, b, 0, 1, 2, 3, 4, 5, 6, 7);
    return __builtin_bit_cast(bf16x8, r);
}
DEVI f32x4 mfma16(bf16x8 a, bf16x8 b, f32x4 c) {
    return __builtin_amdgcn_mfma_f32_16x16x32_bf16(a, b, c, 0, 0, 0); }

template <int N> DEVI void wait_vm() { asm volatile("s_waitcnt vmcnt(%0)" ::"n"(N) : "memory"); }
DEVI void wait_lgkm0() { asm volatile("s_waitcnt lgkmcnt(0)" ::: "memory"); }
DEVI void bar() {
    asm volatile("" ::: "memory");
    __builtin_amdgcn_s_barrier();
    asm volatile("" ::: "memory");
}
DEVI void sched_fence() { __builtin_amdgcn_sched_barrier(0); }

DEVI float dot2_ones(unsigned v, float acc) {
    const bf16x2 ones = {(__bf16)1.0f, (__bf16)1.0f};
    return __builtin_amdgcn_fdot2_f32_bf16(__builtin_bit_cast(bf16x2, v), ones, acc, false);
}
DEVI float dot2_self(unsigned v, float acc) {
    const bf16x2 a = __builtin_bit_cast(bf16x2, v);
    return __builtin_amdgcn_fdot2_f32_bf16(a, a, acc, false);
}
DEVI float frag_sum(bf16x8 f, float acc) {
    u32x4 u = __builtin_bit_cast(u32x4, f);
    acc = dot2_ones(u.x, acc); acc = dot2_ones(u.y, acc); acc = dot2_ones(u.z, acc); acc = dot2_ones(u.w, acc);
    return acc;
}
DEVI float frag_sq(bf16x8 f, float acc) {
    u32x4 u = __builtin_bit_cast(u32x4, f);
    acc = dot2_self(u.x, acc); acc = dot2_self(u.y, acc); acc = dot2_self(u.z, acc); acc = dot2_self(u.w, acc);
    return acc;
}
DEVI float xor16_32_sum(float v) {
    v += __shfl_xor(v, 16);
    v += __shfl_xor(v, 32);
    return v;
}
DEVI float phi_f(float t) { return (t > 0.f ? t + 1.f : __expf(t)) + 1e-6f; }
DEVI float geglu(float g, float u) {
    const float c1 = 2.3022081985f, c2 = 0.1029432397f;
    const float e = __builtin_amdgcn_exp2f(g * fmaf(g * g, -c2, -c1));
    return (g * u) * __builtin_amdgcn_rcpf(1.f + e);
}
DEVI void combine8(const f32x4 (&v)[4], float& mean, float& m2) {
    const float ms = ((v[0][0] + v[0][2]) + (v[1][0] + v[1][2])) + ((v[2][0] + v[2][2]) + (v[3][0] + v[3][2]));
    mean = ms * 0.125f;
    float dd = 0.f, q = 0.f;
#pragma unroll
    for (int i = 0; i < 4; ++i) {
        const float d0 = v[i][0] - mean, d1 = v[i][2] - mean;
        dd += d0 * d0 + d1 * d1;
        q += v[i][1] + v[i][3];
    }
    m2 = q + 64.f * dd;
}
DEVI void asm_load4(const char* sbase, unsigned voff, f32x4 (&v)[4]) {
    asm volatile("global_load_dwordx4 %0, %4, %5\n\tglobal_load_dwordx4 %1, %4, %5 offset:16\n\t"
                 "global_load_dwordx4 %2, %4, %5 offset:32\n\tglobal_load_dwordx4 %3, %4, %5 offset:48"
                 : "=&v"(v[0]), "=&v"(v[1]), "=&v"(v[2]), "=&v"(v[3])
                 : "v"(voff), "s"(sbase)
                 : "memory");
}
template <int N> DEVI void wait_regs4(f32x4 (&v)[4]) {
    asm volatile("s_waitcnt vmcnt(%4)" : "+v"(v[0]), "+v"(v[1]), "+v"(v[2]), "+v"(v[3]) : "n"(N) : "memory");
}

#define STAMP(kid, i)

constexpr size_t WS_PQ = 0;
constexpr size_t WS_O = WS_PQ + (size_t)SEQ * DM * 2;
constexpr size_t WS_H = WS_O + (size_t)SEQ * DM * 2;
constexpr size_t WS_HH = WS_H + (size_t)SEQ * DM * 2;
constexpr size_t WS_KVP = WS_HH + (size_t)SEQ * DF * 2;
constexpr size_t WS_ST = WS_KVP + (size_t)16 * NH * 1056 * 4;
constexpr size_t WS_ST1 = WS_ST + (size_t)SEQ * 16 * 8;
constexpr size_t WS_CQ = WS_ST1 + (size_t)SEQ * 16 * 8;
constexpr size_t WS_CG = WS_CQ + (size_t)16 * 3072 * 4;
constexpr int KVP_STRIDE = 1056;

namespace g1 {
constexpr int BM = 128, BK = 64, S = 4, T = DM / BK;
constexpr int A_BYTES = BM * 128;
constexpr int B_PITCH = 384;
constexpr int B_BYTES = BK * B_PITCH;
constexpr int STAGE = A_BYTES + B_BYTES;
constexpr int NI = 5;
constexpr int LDS_BYTES = S * STAGE;
}

__global__ __launch_bounds__(512, 2) void g1_kernel(const u16* __restrict__ x, const u16* __restrict__ wqkv,
                                                    u16* __restrict__ pq, float* __restrict__ kvp) {
    using namespace g1;
    STAMP(0, 0);
    extern __shared__ __attribute__((aligned(16))) char smem_raw[];
    LAS char* lds = (LAS char*)smem_raw;
    const int tid = threadIdx.x, lane = tid & 63;
    const int w = __builtin_amdgcn_readfirstlane(tid >> 6);
    const int grp = w >> 2, wm = (w >> 1) & 1, wn = w & 1;
    const int bid = blockIdx.x, xcd = bid & 7, loc = bid >> 3;
    const int pm = (xcd & 1) * 8 + (loc & 7);
    const int hp = (xcd >> 1) * 4 + (loc >> 3);
    const int l15 = lane & 15, g = lane >> 4, q4 = (lane >> 2) & 3, p4 = lane & 3;

    unsigned voffA[2], voffB[3];
#pragma unroll
    for (int j = 0; j < 2; ++j) {
        const int i = (j * 8 + w) * 64 + lane, row = i >> 3, c = (i & 7) ^ ((row >> 1) & 7);
        voffA[j] = (unsigned)(row * DM * 2 + c * 16);
    }
#pragma unroll
    for (int j = 0; j < 3; ++j) {
        const int i = (j * 8 + w) * 64 + lane;
        const int row = i / 24, cc = i % 24, blk = cc >> 3, cb = cc & 7;
        const int f = ((row >> 1) & 1) | (((row >> 3) & 1) << 1);
        const int cbs = cb ^ (f << 1);
        voffB[j] = (unsigned)(row * (3 * DM) * 2 + (blk * DM + hp * 64 + cbs * 8) * 2);
    }
    const char* gA = (const char*)x + (size_t)pm * BM * DM * 2;
    const char* gB = (const char*)wqkv;

    const unsigned lds0 = (unsigned)(uintptr_t)lds;
    auto issue = [&](int kt, int buf) {
        const unsigned sb = lds0 + buf * STAGE + w * 1024;
        const char* a = gA + kt * (BK * 2);
        const char* b = gB + (size_t)kt * BK * (3 * DM) * 2;
#pragma unroll
        for (int j = 0; j < 2; ++j) glds16(a, voffA[j], sb + j * 8192);
#pragma unroll
        for (int j = 0; j < 3; ++j) glds16(b, voffB[j], sb + A_BYTES + j * 8192);
    };

    int blk[4];
    blk[0] = 2 * grp + wn; blk[1] = 2 * grp + 1 - wn; blk[2] = 2 * (1 - grp) + wn; blk[3] = 2 * (1 - grp) + 1 - wn;
    int aoff[4];
#pragma unroll
    for (int m = 0; m < 4; ++m) aoff[m] = (64 * wm + 16 * blk[m] + l15) * 128 + 16 * ((4 * grp + g) ^ (l15 >> 1));
    const int fB = ((q4 >> 1) & 1) | ((g & 1) << 1);
    int boff[2];
#pragma unroll
    for (int i = 0; i < 2; ++i)
        boff[i] = A_BYTES + (32 * grp + 8 * g + q4) * B_PITCH + 32 * ((2 * wn + (i ^ wm)) ^ fB) + 8 * p4;

    f32x4 acc[4][6];
#pragma unroll
    for (int m = 0; m < 4; ++m)
#pragma unroll
        for (int n = 0; n < 6; ++n) acc[m][n] = (f32x4){0.f, 0.f, 0.f, 0.f};
    float rs[2] = {0.f, 0.f}, rq[2] = {0.f, 0.f}, cs[3] = {0.f, 0.f, 0.f};

    auto step = [&](int t, auto issue_tag, auto vm_tag) {
        constexpr bool ISSUE = decltype(issue_tag)::value;
        constexpr int VM = decltype(vm_tag)::value;
        LAS char* sb = lds + (t & (S - 1)) * STAGE;
        bf16x8 a[4], b[6];
#pragma unroll
        for (int n = 0; n < 6; ++n) {
            LAS char* pb = sb + boff[n & 1] + (n >> 1) * 128;
            b[n] = lds_read_tr2(pb, pb + 4 * B_PITCH);
        }
#pragma unroll
        for (int m = 0; m < 4; ++m) a[m] = lds_read_b128(sb + aoff[m]);
        if constexpr (ISSUE) issue(t + S - 1, (t + S - 1) & (S - 1));
        cs[0] = frag_sum(b[0], cs[0]); cs[1] = frag_sum(b[2], cs[1]); cs[2] = frag_sum(b[4], cs[2]);
        rs[0] = frag_sum(a[0], rs[0]); rq[0] = frag_sq(a[0], rq[0]);
        rs[1] = frag_sum(a[2], rs[1]); rq[1] = frag_sq(a[2], rq[1]);
        if (grp == 1) wait_vm<VM>();
        wait_lgkm0();
        sched_fence();
        bar();
        sched_fence();
        __builtin_amdgcn_s_setprio(1);
#pragma unroll
        for (int m = 0; m < 4; ++m) {
            acc[m][0] = mfma16(b[0], a[m], acc[m][0]);
            acc[m][1] = mfma16(b[1], a[m], acc[m][1]);
#pragma unroll
            for (int n = 2; n < 6; ++n) acc[m][n] = mfma16(a[m], b[n], acc[m][n]);
        }
        __builtin_amdgcn_s_setprio(0);
        if (grp == 0) wait_vm<VM>();
        sched_fence();
        bar();
        sched_fence();
    };

#pragma unroll
    for (int s = 0; s < S - 1; ++s) issue(s, s);
    wait_vm<(S - 2) * NI>();
    bar();
    STAMP(0, 1);
    if (grp == 1) bar();
    for (int t = 0; t <= T - S; ++t) step(t, std::true_type{}, std::integral_constant<int, (S - 2) * NI>{});
    step(T - 3, std::false_type{}, std::integral_constant<int, 1 * NI>{});
    step(T - 2, std::false_type{}, std::integral_constant<int, 0>{});
    step(T - 1, std::false_type{}, std::integral_constant<int, 0>{});
    if (grp == 0) bar();
    STAMP(0, 2);

    LAS char* xch = lds;
    LAS float* rowstat = (LAS float*)(lds + 98304);
    LAS float* colsum = (LAS float*)(lds + 98304 + 2048);
    {
        LAS char* dst = xch + (w ^ 4) * 12288 + lane * 16;
#pragma unroll
        for (int mm = 0; mm < 2; ++mm)
#pragma unroll
            for (int n = 0; n < 6; ++n) *(LAS f32x4*)(dst + (mm * 6 + n) * 1024) = acc[2 + mm][n];
#pragma unroll
        for (int i = 0; i < 2; ++i) {
            const float s = xor16_32_sum(rs[i]), s2 = xor16_32_sum(rq[i]);
            if (lane < 16) {
                const int row = 64 * wm + 16 * blk[2 * i] + l15;
                rowstat[(grp * 128 + row) * 2] = s;
                rowstat[(grp * 128 + row) * 2 + 1] = s2;
            }
        }
#pragma unroll
        for (int i = 0; i < 3; ++i) {
            const float s = xor16_32_sum(cs[i]);
            if (lane < 16) colsum[grp * 192 + i * 64 + wn * 32 + wm * 16 + l15] = s;
        }
    }
    wait_lgkm0();
    bar();
    STAMP(0, 3);
    f32x4 fin[2][6];
    {
        LAS char* src = xch + w * 12288 + lane * 16;
#pragma unroll
        for (int mm = 0; mm < 2; ++mm)
#pragma unroll
            for (int n = 0; n < 6; ++n) {
                f32x4 v = *(LAS f32x4*)(src + (mm * 6 + n) * 1024);
                fin[mm][n] = v + acc[mm][n];
            }
    }
    auto row_stats = [&](int row, float& mean, float& rstd) {
        const float s = rowstat[row * 2] + rowstat[(128 + row) * 2];
        const float s2 = rowstat[row * 2 + 1] + rowstat[(128 + row) * 2 + 1];
        mean = s * (1.f / DM);
        const float var = fmaxf(s2 * (1.f / DM) - mean * mean, 0.f);
        rstd = rsqrtf(var + LN_EPS);
    };
#pragma unroll
    for (int mm = 0; mm < 2; ++mm) {
        const int row = 64 * wm + 16 * blk[mm] + l15;
        float mean, rstd; row_stats(row, mean, rstd);
#pragma unroll
        for (int i = 0; i < 2; ++i) {
            const int c0 = wn * 32 + (i ^ wm) * 16 + 4 * g;
            float v[4];
#pragma unroll
            for (int j = 0; j < 4; ++j) {
                const float cw = colsum[c0 + j] + colsum[192 + c0 + j];
                v[j] = phi_f(round_bf16(rstd * (fin[mm][i][j] - mean * cw)));
            }
            u32x2 o; o.x = pack_bf16(v[0], v[1]); o.y = pack_bf16(v[2], v[3]);
            *(u32x2*)(pq + (size_t)(pm * BM + row) * DM + hp * 64 + c0) = o;
        }
    }
    STAMP(0, 4);
    float mean_t[2][4], rstd_t[2][4];
#pragma unroll
    for (int mm = 0; mm < 2; ++mm)
#pragma unroll
        for (int j = 0; j < 4; ++j) row_stats(64 * wm + 16 * blk[mm] + 4 * g + j, mean_t[mm][j], rstd_t[mm][j]);
    bf16x8 pk[2], pv[2];
#pragma unroll
    for (int i = 0; i < 2; ++i) {
        const int cc = wn * 32 + (i ^ wm) * 16 + l15;
        const float cwk = colsum[64 + cc] + colsum[192 + 64 + cc];
        const float cwv = colsum[128 + cc] + colsum[192 + 128 + cc];
#pragma unroll
        for (int mm = 0; mm < 2; ++mm)
#pragma unroll
            for (int j = 0; j < 4; ++j) {
                const float kk = round_bf16(rstd_t[mm][j] * (fin[mm][2 + i][j] - mean_t[mm][j] * cwk));
                const float vv = rstd_t[mm][j] * (fin[mm][4 + i][j] - mean_t[mm][j] * cwv);
                pk[i][mm * 4 + j] = (__bf16)phi_f(kk);
                pv[i][mm * 4 + j] = (__bf16)vv;
            }
    }
    bf16x8 ones;
#pragma unroll
    for (int j = 0; j < 8; ++j) ones[j] = (__bf16)1.0f;
    LAS float* red = (LAS float*)(lds + 102400);
    {
        LAS float* mine = red + w * KVP_STRIDE;
#pragma unroll
        for (int i = 0; i < 2; ++i) {
#pragma unroll
            for (int i2 = 0; i2 < 2; ++i2) {
                f32x4 d = mfma16(pk[i], pv[i2], (f32x4){0.f, 0.f, 0.f, 0.f});
                *(LAS f32x4*)(mine + (16 * (i2 ^ wm) + l15) * 32 + 16 * (i ^ wm) + 4 * g) = d;
            }
            f32x4 z = mfma16(pk[i], ones, (f32x4){0.f, 0.f, 0.f, 0.f});
            if (l15 == 0) *(LAS f32x4*)(mine + 1024 + 16 * (i ^ wm) + 4 * g) = z;
        }
    }
    wait_lgkm0();
    bar();
    STAMP(0, 5);
    for (int idx = tid; idx < 2 * 264; idx += 512) {
        const int hh = idx / 264, e4 = idx % 264;
        f32x4 s = *(LAS f32x4*)(red + (hh + 0) * KVP_STRIDE + e4 * 4);
        s += *(LAS f32x4*)(red + (hh + 2) * KVP_STRIDE + e4 * 4);
        s += *(LAS f32x4*)(red + (hh + 4) * KVP_STRIDE + e4 * 4);
        s += *(LAS f32x4*)(red + (hh + 6) * KVP_STRIDE + e4 * 4);
        *(f32x4*)(kvp + ((size_t)pm * NH + hp * 2 + hh) * KVP_STRIDE + e4 * 4) = s;
    }
    STAMP(0, 6);
    STAMP(0, 7);
}

__global__ __launch_bounds__(256) void k2_kernel(const u16* __restrict__ pq, const float* __restrict__ kvp,
                                                 u16* __restrict__ o) {
    __shared__ __attribute__((aligned(16))) u16 kvT[32 * 32 + 32];
    const int tid = threadIdx.x, lane = tid & 63, w = tid >> 6;
    const int h = blockIdx.x & 31, rc = blockIdx.x >> 5;
    const int l15 = lane & 15, g = lane >> 4;
    const int row0 = rc * 256 + w * 64;
    bf16x8 pf[4];
#pragma unroll
    for (int m = 0; m < 4; ++m)
        pf[m] = *(const bf16x8*)(pq + (size_t)(row0 + 16 * m + l15) * DM + h * 32 + 8 * g);
    {
        f32x4 s = {0.f, 0.f, 0.f, 0.f};
        f32x4 v[16];
#pragma unroll
        for (int p = 0; p < 16; ++p) v[p] = *(const f32x4*)(kvp + ((size_t)p * NH + h) * KVP_STRIDE + tid * 4);
#pragma unroll
        for (int p = 0; p < 16; ++p) s += v[p];
        u32x2 r; r.x = pack_bf16(s[0], s[1]); r.y = pack_bf16(s[2], s[3]);
        *(u32x2*)(kvT + tid * 4) = r;
        if (tid < 8) {
            f32x4 z = {0.f, 0.f, 0.f, 0.f};
#pragma unroll
            for (int p = 0; p < 16; ++p) z += *(const f32x4*)(kvp + ((size_t)p * NH + h) * KVP_STRIDE + 1024 + tid * 4);
            u32x2 rz; rz.x = pack_bf16(z[0], z[1]); rz.y = pack_bf16(z[2], z[3]);
            *(u32x2*)(kvT + 1024 + tid * 4) = rz;
        }
    }
    __syncthreads();
    const int e0 = 8 * (l15 >> 2) + (l15 & 3);
    const bf16x8 a0 = *(const bf16x8*)(kvT + e0 * 32 + 8 * g);
    const bf16x8 a1 = *(const bf16x8*)(kvT + (e0 + 4) * 32 + 8 * g);
    const bf16x8 az = *(const bf16x8*)(kvT + 1024 + 8 * g);
    const f32x4 zero = {0.f, 0.f, 0.f, 0.f};
#pragma unroll
    for (int m = 0; m < 4; ++m) {
        const f32x4 n0 = mfma16(a0, pf[m], zero), n1 = mfma16(a1, pf[m], zero), dn = mfma16(az, pf[m], zero);
        const float inv = 1.f / (dn[0] + 1e-6f);
        u32x4 r;
        r.x = pack_bf16(n0[0] * inv, n0[1] * inv); r.y = pack_bf16(n0[2] * inv, n0[3] * inv);
        r.z = pack_bf16(n1[0] * inv, n1[1] * inv); r.w = pack_bf16(n1[2] * inv, n1[3] * inv);
        *(u32x4*)(o + (size_t)(row0 + 16 * m + l15) * DM + h * 32 + 8 * g) = r;
    }
}

namespace g24 {
constexpr int BM = 128, BN = 64, BK = 128, S = 3;
constexpr int A_BYTES = BM * 256;
constexpr int B_BYTES = BK * 128;
constexpr int STAGE = A_BYTES + B_BYTES;
constexpr int NI = 6;
constexpr int LDS_BYTES = S * STAGE;
}

template <int KDIM, bool STATS>
__global__ __launch_bounds__(512, 2) void g24_kernel(const u16* __restrict__ A, const u16* __restrict__ B,
                                                     const u16* __restrict__ res, u16* __restrict__ out,
                                                     float* __restrict__ stats) {
    using namespace g24;
    constexpr int T = KDIM / BK;
    constexpr int KID = STATS ? 2 : 4;
    STAMP(KID, 0);
    extern __shared__ __attribute__((aligned(16))) char smem_raw[];
    LAS char* lds = (LAS char*)smem_raw;
    const int tid = threadIdx.x, lane = tid & 63;
    const int w = __builtin_amdgcn_readfirstlane(tid >> 6);
    const int grp = w >> 2, kq = w >> 1, wm = w & 1;
    const int bid = blockIdx.x, xcd = bid & 7, loc = bid >> 3;
    const int pm = (xcd & 3) * 4 + (loc & 3);
    const int pn = (xcd >> 2) * 8 + (loc >> 2);
    const int l15 = lane & 15, g = lane >> 4, q4 = (lane >> 2) & 3, p4 = lane & 3;

    unsigned voffA[4], voffB[2];
#pragma unroll
    for (int j = 0; j < 4; ++j) {
        const int i = (j * 8 + w) * 64 + lane, row = i >> 4, c = (i & 15) ^ (row & 15);
        voffA[j] = (unsigned)(row * KDIM * 2 + c * 16);
    }
#pragma unroll
    for (int j = 0; j < 2; ++j) {
        const int i = (j * 8 + w) * 64 + lane, row = i >> 3, cb = i & 7;
        const int f = ((row >> 1) & 1) | (((row >> 3) & 1) << 1);
        voffB[j] = (unsigned)(row * DM * 2 + (pn * 64 + (cb ^ (f << 1)) * 8) * 2);
    }
    const char* gA = (const char*)A + (size_t)pm * BM * KDIM * 2;
    const char* gB = (const char*)B;
    const unsigned lds0 = (unsigned)(uintptr_t)lds;
    auto issue = [&](int kt, int buf) {
        const unsigned sb = lds0 + buf * STAGE + w * 1024;
        const char* a = gA + kt * (BK * 2);
        const char* b = gB + (size_t)kt * BK * DM * 2;
#pragma unroll
        for (int j = 0; j < 4; ++j) glds16(a, voffA[j], sb + j * 8192);
#pragma unroll
        for (int j = 0; j < 2; ++j) glds16(b, voffB[j], sb + A_BYTES + j * 8192);
    };
    int aoff[4];
#pragma unroll
    for (int m = 0; m < 4; ++m) aoff[m] = (64 * wm + 16 * ((m + kq) & 3) + l15) * 256 + 16 * ((4 * kq + g) ^ l15);
    const int fB = ((q4 >> 1) & 1) | ((g & 1) << 1);
    int boff[4];
#pragma unroll
    for (int i = 0; i < 4; ++i) boff[i] = A_BYTES + (32 * kq + 8 * g + q4) * 128 + 32 * (i ^ fB) + 8 * p4;

    f32x4 acc[4][4];
#pragma unroll
    for (int m = 0; m < 4; ++m)
#pragma unroll
        for (int n = 0; n < 4; ++n) acc[m][n] = (f32x4){0.f, 0.f, 0.f, 0.f};

    int buf_rd = 0, buf_wr = S - 1;
    auto step = [&](int t, auto issue_tag, auto vm_tag) {
        constexpr bool ISSUE = decltype(issue_tag)::value;
        constexpr int VM = decltype(vm_tag)::value;
        LAS char* sb = lds + buf_rd * STAGE;
        bf16x8 a[4], b[4];
#pragma unroll
        for (int n = 0; n < 4; ++n) b[n] = lds_read_tr2(sb + boff[n], sb + boff[n] + 4 * 128);
#pragma unroll
        for (int m = 0; m < 4; ++m) a[m] = lds_read_b128(sb + aoff[m]);
        if constexpr (ISSUE) issue(t + S - 1, buf_wr);
        buf_rd = buf_rd == S - 1 ? 0 : buf_rd + 1;
        buf_wr = buf_wr == S - 1 ? 0 : buf_wr + 1;
        if (grp == 1) wait_vm<VM>();
        wait_lgkm0();
        sched_fence();
        bar();
        sched_fence();
        __builtin_amdgcn_s_setprio(1);
#pragma unroll
        for (int m = 0; m < 4; ++m)
#pragma unroll
            for (int n = 0; n < 4; ++n) acc[m][n] = mfma16(b[n], a[m], acc[m][n]);
        __builtin_amdgcn_s_setprio(0);
        if (grp == 0) wait_vm<VM>();
        sched_fence();
        bar();
        sched_fence();
    };
#pragma unroll
    for (int s = 0; s < S - 1; ++s) issue(s, s);
    wait_vm<(S - 2) * NI>();
    bar();
    STAMP(KID, 1);
    if (grp == 1) bar();
    for (int t = 0; t <= T - S; ++t) step(t, std::true_type{}, std::integral_constant<int, (S - 2) * NI>{});
    step(T - 2, std::false_type{}, std::integral_constant<int, 0>{});
    step(T - 1, std::false_type{}, std::integral_constant<int, 0>{});
    if (grp == 0) bar();
    STAMP(KID, 2);

    {
#pragma unroll
        for (int m = 1; m < 4; ++m) {
            const int dw = ((m + kq) & 3) * 2 + wm;
            LAS char* dst = lds + ((dw * 3 + (m - 1)) * 4) * 1024 + lane * 16;
#pragma unroll
            for (int n = 0; n < 4; ++n) *(LAS f32x4*)(dst + n * 1024) = acc[m][n];
        }
    }
    const int row = pm * BM + 64 * wm + 16 * kq + l15;
    u32x2 rv[4];
#pragma unroll
    for (int n = 0; n < 4; ++n) rv[n] = *(const u32x2*)(res + (size_t)row * DM + pn * 64 + 16 * n + 4 * g);
    wait_lgkm0();
    bar();
    STAMP(KID, 3);
    f32x4 fin[4];
#pragma unroll
    for (int n = 0; n < 4; ++n) fin[n] = acc[0][n];
#pragma unroll
    for (int s = 0; s < 3; ++s) {
        LAS char* src = lds + ((w * 3 + s) * 4) * 1024 + lane * 16;
#pragma unroll
        for (int n = 0; n < 4; ++n) fin[n] += *(LAS f32x4*)(src + n * 1024);
    }
    float hv[4][4];
#pragma unroll
    for (int n = 0; n < 4; ++n) {
        hv[n][0] = round_bf16(bf_lo(rv[n].x) + round_bf16(fin[n][0]));
        hv[n][1] = round_bf16(bf_hi(rv[n].x) + round_bf16(fin[n][1]));
        hv[n][2] = round_bf16(bf_lo(rv[n].y) + round_bf16(fin[n][2]));
        hv[n][3] = round_bf16(bf_hi(rv[n].y) + round_bf16(fin[n][3]));
        u32x2 o; o.x = pack_bf16(hv[n][0], hv[n][1]); o.y = pack_bf16(hv[n][2], hv[n][3]);
        *(u32x2*)(out + (size_t)row * DM + pn * 64 + 16 * n + 4 * g) = o;
    }
    if constexpr (STATS) {
        float s = 0.f;
#pragma unroll
        for (int n = 0; n < 4; ++n) s += (hv[n][0] + hv[n][1]) + (hv[n][2] + hv[n][3]);
        s = xor16_32_sum(s);
        const float mu = s * (1.f / 64.f);
        float m2 = 0.f;
#pragma unroll
        for (int n = 0; n < 4; ++n)
#pragma unroll
            for (int j = 0; j < 4; ++j) { const float d = hv[n][j] - mu; m2 += d * d; }
        m2 = xor16_32_sum(m2);
        if (lane < 16) *(f32x2*)(stats + ((size_t)row * 16 + pn) * 2) = (f32x2){mu, m2};
    }
    STAMP(KID, 6);
    STAMP(KID, 7);
}

namespace g3 {
constexpr int BM = 256, NU = DM / 32;
constexpr int A_PIECE = 16384, B_SUB = 32 * 224, B_PIECE = 2 * B_SUB;
constexpr int NR = 5;
constexpr int B_OFF = NR * A_PIECE;
constexpr int TAB_OFF = B_OFF + NR * B_PIECE;
constexpr int LDS_BYTES = TAB_OFF + 2048 + 1024;
constexpr int OPITCH = 240;
}

__global__ __launch_bounds__(512, 2) void g3_kernel(const u16* __restrict__ h, const u16* __restrict__ wg,
                                                    const u16* __restrict__ wu, const float* __restrict__ stats,
                                                    const float* __restrict__ colpart, u16* __restrict__ hh) {
    using namespace g3;
    STAMP(3, 0);
    extern __shared__ __attribute__((aligned(16))) char smem_raw[];
    LAS char* lds = (LAS char*)smem_raw;
    const int tid = threadIdx.x, lane = tid & 63;
    const int w = __builtin_amdgcn_readfirstlane(tid >> 6);
    const int grp = w >> 2, wm = w >> 1, wn = w & 1;
    const int bid = blockIdx.x, xcd = bid & 7, loc = bid >> 3;
    const int pm = (xcd & 1) * 4 + (loc & 3);
    const int pn = (xcd >> 1) * 8 + (loc >> 2);
    const int l15 = lane & 15, g = lane >> 4, q4 = (lane >> 2) & 3, p4 = lane & 3;
    const unsigned lds0 = (unsigned)(uintptr_t)lds;

    f32x4 sv[4];
    asm_load4((const char*)stats + (size_t)pm * BM * 128, (unsigned)tid * 64u, sv);
    f32x4 cv[4];
    {
        const int tc = min(tid, 223);
        asm_load4((const char*)colpart + (size_t)pn * 112 * 64, (unsigned)(tc >= 112 ? DF + tc - 112 : tc) * 64u, cv);
    }

    unsigned voffA[2], voffB[2];
    const char* gB[2];
    const int bI = min(2 * w, 12);
#pragma unroll
    for (int j = 0; j < 2; ++j) {
        const int b = bI + j, sub = b >= 7 ? 1 : 0;
        const int i = (b - 7 * sub) * 64 + lane, rho = i / 14, cc = i % 14;
        const int r = (rho & ~12) | ((rho & 4) << 1) | ((rho & 8) >> 1);
        voffB[j] = (unsigned)(r * DF * 2 + (pn * 112 + cc * 8) * 2);
        gB[j] = (const char*)(sub ? wu : wg) - j * 1024;
        const int ia = (2 * w + j) * 64 + lane, row = ia >> 3, c = (ia & 7) ^ ((row >> 1) & 7);
        voffA[j] = (unsigned)(row * DM * 2 + c * 16 - j * 1024);
    }
    const char* gA = (const char*)h + (size_t)pm * BM * DM * 2;
    const unsigned ldsA = lds0 + 2 * w * 1024, ldsB = lds0 + B_OFF + bI * 1024;
    constexpr unsigned BSTEP = 32 * DF * 2, AHALF = 128 * DM * 2;
    auto issueB = [&](int p, int slot) {
        glds16x2(gB[0] + (size_t)p * BSTEP, voffB[0], gB[1] + (size_t)p * BSTEP, voffB[1], ldsB + slot * B_PIECE);
    };
    auto issueA = [&](int p, int slot) {
        const char* a = gA + (size_t)(p & 1) * AHALF + (p >> 1) * 128;
        glds16x2(a, voffA[0], a, voffA[1], ldsA + slot * A_PIECE);
    };
    issueB(0, 0); issueA(0, 0); issueA(1, 1); issueB(1, 1);

    LAS f32x2* rstat = (LAS f32x2*)(lds + TAB_OFF);
    LAS float* colsum = (LAS float*)(lds + TAB_OFF + 2048);
    {
        wait_regs4<8>(sv);
        wait_regs4<8>(cv);
        const f32x4 c4 = (cv[0] + cv[1]) + (cv[2] + cv[3]);
        if (tid < 224) colsum[tid] = (c4[0] + c4[1]) + (c4[2] + c4[3]);
        float mean, m2;
        combine8(sv, mean, m2);
        const float mo = __shfl_xor(mean, 1), m2o = __shfl_xor(m2, 1);
        const float d = mean - mo;
        const float var = (m2 + m2o + 256.f * d * d) * (1.f / DM);
        if ((tid & 1) == 0) rstat[tid >> 1] = (f32x2){0.5f * (mean + mo), rsqrtf(var + LN_EPS)};
    }
    issueA(2, 2); issueB(2, 2);
    if (grp == 1) issueA(3, 3);
    issueB(3, 3);

    int aoff[2];
#pragma unroll
    for (int ks = 0; ks < 2; ++ks) aoff[ks] = (64 * (wm & 1) + l15) * 128 + 16 * ((4 * ks + g) ^ (l15 >> 1));
    const int brow = (16 * (g >> 1) + 4 * (g & 1) + q4) * 224;
    const int boff = B_OFF + brow + 112 * wn + 8 * p4;
    const int boffX = B_OFF + brow + 112 * wn + 96 + (p4 < 2 ? 8 * p4 : B_SUB + 8 * (p4 - 2));

    f32x4 acc[4][7];
#pragma unroll
    for (int m = 0; m < 4; ++m)
#pragma unroll
        for (int n = 0; n < 7; ++n) acc[m][n] = (f32x4){0.f, 0.f, 0.f, 0.f};

    unsigned voffAe[2], voffAo[2], voffBo[2];
#pragma unroll
    for (int j = 0; j < 2; ++j) {
        voffAe[j] = voffA[j] + (grp ? 256u : AHALF + 128u);
        voffAo[j] = voffA[j] + (grp ? AHALF + 256u : 256u);
        voffBo[j] = voffB[j] + BSTEP;
    }
    const char* pA = gA;
    const char* pB0 = gB[0] + (size_t)4 * BSTEP;
    const char* pB1 = gB[1] + (size_t)4 * BSTEP;
    int rdB = 0, prevB = 4 * B_PIECE;
    int rdA = grp * A_PIECE;
    int vB = boff, vBX = boffX, vA0 = aoff[0] + rdA, vA1 = aoff[1] + rdA;
    auto ringA = [&](int x, int k) { x += k * A_PIECE; return x >= NR * A_PIECE ? x - NR * A_PIECE : x; };

    auto unit = [&](auto ks_tag, auto iss_tag, auto wv_tag) {
        constexpr int ks = decltype(ks_tag)::value, ISS = decltype(iss_tag)::value, WV = decltype(wv_tag)::value;
        bf16x8 a[4], b[7];
#pragma unroll
        for (int n = 0; n < 6; ++n) {
            LAS char* pb = lds + vB + (n >= 3 ? B_SUB : 0) + (n % 3) * 32;
            b[n] = lds_read_tr2(pb, pb + 8 * 224);
        }
        { LAS char* pb = lds + vBX; b[6] = lds_read_tr2(pb, pb + 8 * 224); }
#pragma unroll
        for (int m = 0; m < 4; ++m) a[m] = lds_read_b128(lds + (ks ? vA1 : vA0) + m * 2048);
        sched_fence();
        const unsigned dA = ldsA + ringA(rdA, 3 + ks), dB = ldsB + prevB;
        const char* qA = pA; const char* qB0 = pB0; const char* qB1 = pB1;
        if constexpr (ISS == 1) {
            if constexpr (G3VAR < 2) glds16x2(qA, ks ? voffAo[0] : voffAe[0], qA, ks ? voffAo[1] : voffAe[1], dA);
            if constexpr (G3VAR < 1) glds16x2(qB0, ks ? voffBo[0] : voffB[0], qB1, ks ? voffBo[1] : voffB[1], dB);
            if constexpr (ks == 1) { pA += 128; pB0 += 2 * BSTEP; pB1 += 2 * BSTEP; }
        } else if constexpr (ISS == 2) {
            if (grp == 0) glds16x2(qA, voffAe[0], qA, voffAe[1], dA);
        }
        prevB = rdB;
        rdB = rdB == (NR - 1) * B_PIECE ? 0 : rdB + B_PIECE;
        vB = boff + rdB; vBX = boffX + rdB;
        if constexpr (ks == 1) { rdA = ringA(rdA, 2); vA0 = aoff[0] + rdA; vA1 = aoff[1] + rdA; }
        sched_fence();
        if constexpr (WV >= 0) wait_vm<WV>();
        wait_lgkm0();
        sched_fence();
        bar();
        sched_fence();
        __builtin_amdgcn_s_setprio(1);
#pragma unroll
        for (int m = 0; m < 4; ++m) {
#pragma unroll
            for (int n = 0; n < 7; ++n) {
                acc[m][n] = mfma16(b[n], a[m], acc[m][n]);
                if constexpr (ISS == 1 && G3VAR == 2) {
                    if (n == 3) {
                        sched_fence();
                        if (m == 0) glds16o<0>(qA, ks ? voffAo[0] : voffAe[0], dA);
                        if (m == 1) glds16o<1024>(qA, ks ? voffAo[1] : voffAe[1], dA);
                        if (m == 2) glds16o<0>(qB0, ks ? voffBo[0] : voffB[0], dB);
                        if (m == 3) glds16o<1024>(qB1, ks ? voffBo[1] : voffB[1], dB);
                        sched_fence();
                    }
                }
                if constexpr (ISS == 1 && G3VAR == 1) {
                    if (n == 6 && (m == 0 || m == 2)) {
                        sched_fence();
                        if (m == 0) glds16o<0>(qB0, ks ? voffBo[0] : voffB[0], dB);
                        if (m == 2) glds16o<1024>(qB1, ks ? voffBo[1] : voffB[1], dB);
                        sched_fence();
                    }
                }
            }
        }
        __builtin_amdgcn_s_setprio(0);
        sched_fence();
        bar();
        sched_fence();
    };
    #define IC(n) std::integral_constant<int, (n)>{}
    if (grp == 1) wait_vm<10>(); else wait_vm<10>();
    wait_lgkm0();
    bar();
    STAMP(3, 1);
    if (grp == 1) bar();
    for (int t = 0; t < 14; ++t) {
        unit(IC(0), IC(1), IC(10 - 2 * G3VAR));
        unit(IC(1), IC(1), IC(10 - 2 * G3VAR));
    }
    unit(IC(0), IC(2), IC(8));
    unit(IC(1), IC(0), IC(2));
    unit(IC(0), IC(0), IC(0));
    unit(IC(1), IC(0), IC(-1));
    if (grp == 0) bar();
    STAMP(3, 2);

    f32x4 cw[7];
#pragma unroll
    for (int n = 0; n < 6; ++n) cw[n] = *(LAS f32x4*)(colsum + (n >= 3 ? 112 : 0) + 56 * wn + 16 * (n % 3) + 4 * g);
    cw[6] = *(LAS f32x4*)(colsum + (g >= 2 ? 112 : 0) + 56 * wn + 48 + 4 * (g & 1));
    f32x2 st[4];
#pragma unroll
    for (int m = 0; m < 4; ++m) st[m] = rstat[64 * wm + 16 * m + l15];
    LAS char* orow = lds + (64 * wm + l15) * OPITCH + 112 * wn + 8 * g;
#pragma unroll
    for (int m = 0; m < 4; ++m) {
#pragma unroll
        for (int n = 0; n < 3; ++n) {
            const f32x4 gg = (acc[m][n] - st[m].x * cw[n]) * st[m].y;
            const f32x4 uu = (acc[m][n + 3] - st[m].x * cw[n + 3]) * st[m].y;
            u32x2 o;
            o.x = pack_bf16(geglu(gg[0], uu[0]), geglu(gg[1], uu[1]));
            o.y = pack_bf16(geglu(gg[2], uu[2]), geglu(gg[3], uu[3]));
            *(LAS u32x2*)(orow + m * 16 * OPITCH + 32 * n) = o;
        }
    }
#pragma unroll
    for (int mp = 0; mp < 2; ++mp) {
        const f32x4 y0 = (acc[2 * mp][6] - st[2 * mp].x * cw[6]) * st[2 * mp].y;
        const f32x4 y1 = (acc[2 * mp + 1][6] - st[2 * mp + 1].x * cw[6]) * st[2 * mp + 1].y;
        float v[4];
#pragma unroll
        for (int j = 0; j < 4; ++j) {
            auto r = __builtin_amdgcn_permlane32_swap(__float_as_uint(y0[j]), __float_as_uint(y1[j]), false, false);
            v[j] = geglu(__uint_as_float(r[0]), __uint_as_float(r[1]));
        }
        u32x2 o; o.x = pack_bf16(v[0], v[1]); o.y = pack_bf16(v[2], v[3]);
        *(LAS u32x2*)(lds + (64 * wm + 16 * (2 * mp + (lane >> 5)) + l15) * OPITCH + 112 * wn + 96 + 8 * (g & 1)) = o;
    }
    wait_lgkm0();
    bar();
    STAMP(3, 3);
    {
        int row = tid / 14, cc = tid % 14;
        char* gdst = (char*)hh + ((size_t)pm * BM * DF + pn * 112) * 2;
#pragma unroll
        for (int j = 0; j < 7; ++j) {
            const u32x4 v = *(LAS u32x4*)(lds + row * OPITCH + cc * 16);
            *(u32x4*)(gdst + (unsigned)(row * (DF * 2) + cc * 16)) = v;
            row += 36; cc += 8;
            if (cc >= 14) { cc -= 14; row += 1; }
        }
    }
    STAMP(3, 6);
    STAMP(3, 7);
}

__global__ __launch_bounds__(256) void k0_kernel(const u16* __restrict__ x, const u16* __restrict__ wqkv,
                                                 const u16* __restrict__ wg, const u16* __restrict__ wu,
                                                 float* __restrict__ st1, float* __restrict__ cq, float* __restrict__ cg) {
    const int tid = threadIdx.x, bid = blockIdx.x;
    if (bid < 256) {
        const int row = bid * 8 + (tid >> 5), c0 = (tid & 31) * 32;
        float v[32];
#pragma unroll
        for (int i = 0; i < 4; ++i) {
            const u32x4 u = *(const u32x4*)(x + (size_t)row * DM + c0 + i * 8);
#pragma unroll
            for (int j = 0; j < 4; ++j) { v[i * 8 + 2 * j] = bf_lo(u[j]); v[i * 8 + 2 * j + 1] = bf_hi(u[j]); }
        }
        float s = 0.f;
#pragma unroll
        for (int i = 0; i < 32; ++i) s += v[i];
        s += __shfl_xor(s, 1);
        const float mu = s * (1.f / 64.f);
        float m2 = 0.f;
#pragma unroll
        for (int i = 0; i < 32; ++i) { const float d = v[i] - mu; m2 += d * d; }
        m2 += __shfl_xor(m2, 1);
        if ((tid & 1) == 0) *(f32x2*)(st1 + ((size_t)row * 16 + ((tid & 31) >> 1)) * 2) = (f32x2){mu, m2};
    } else {
        int b = bid - 256;
        const u16* W; int N; float* out; int kc, cgp;
        if (b < 32) { W = wqkv; N = 3072; kc = b >> 1; cgp = b & 1; out = cq + kc; }
        else if (b < 64) { b -= 32; W = wg; N = DF; kc = b >> 1; cgp = b & 1; out = cg + kc; }
        else { b -= 64; W = wu; N = DF; kc = b >> 1; cgp = b & 1; out = cg + (size_t)DF * 16 + kc; }
        const int c0 = cgp * 2048 + tid * 8;
        if (c0 < N) {
            float s[8] = {0.f, 0.f, 0.f, 0.f, 0.f, 0.f, 0.f, 0.f};
#pragma unroll 16
            for (int k = 0; k < 64; ++k) {
                const u32x4 u = *(const u32x4*)(W + (size_t)(kc * 64 + k) * N + c0);
#pragma unroll
                for (int j = 0; j < 4; ++j) { s[2 * j] += bf_lo(u[j]); s[2 * j + 1] += bf_hi(u[j]); }
            }
#pragma unroll
            for (int j = 0; j < 8; ++j) out[(size_t)(c0 + j) * 16] = s[j];
        }
    }
}

static void run_stage(int stage, void* const* d_in, void* d_out, void* d_ws, hipStream_t stream) {
    const u16* x = (const u16*)d_in[0];
    const u16* wqkv = (const u16*)d_in[1];
    const u16* wo = (const u16*)d_in[2];
    const u16* wg = (const u16*)d_in[4];
    const u16* wu = (const u16*)d_in[5];
    const u16* wd = (const u16*)d_in[6];
    char* ws = (char*)d_ws;
    u16* pq = (u16*)(ws + WS_PQ);
    u16* o = (u16*)(ws + WS_O);
    u16* h = (u16*)(ws + WS_H);
    u16* hh = (u16*)(ws + WS_HH);
    float* kvp = (float*)(ws + WS_KVP);
    float* st = (float*)(ws + WS_ST);
    float* st1 = (float*)(ws + WS_ST1);
    float* cq = (float*)(ws + WS_CQ);
    float* cg = (float*)(ws + WS_CG);
    u16* out = (u16*)d_out;
    switch (stage) {
    case 5:
        k0_kernel<<<256 + 96, 256, 0, stream>>>(x, wqkv, wg, wu, st1, cq, cg);
        break;
    case 0:
        (void)hipFuncSetAttribute((const void*)g1_kernel, hipFuncAttributeMaxDynamicSharedMemorySize, g1::LDS_BYTES);
        g1_kernel<<<256, 512, g1::LDS_BYTES, stream>>>(x, wqkv, pq, kvp);
        break;
    case 1:
        k2_kernel<<<256, 256, 0, stream>>>(pq, kvp, o);
        break;
    case 2:
        (void)hipFuncSetAttribute((const void*)g24_kernel<DM, true>, hipFuncAttributeMaxDynamicSharedMemorySize, g24::LDS_BYTES);
        g24_kernel<DM, true><<<256, 512, g24::LDS_BYTES, stream>>>(o, wo, x, h, st);
        break;
    case 3:
        (void)hipFuncSetAttribute((const void*)g3_kernel, hipFuncAttributeMaxDynamicSharedMemorySize, g3::LDS_BYTES);
        g3_kernel<<<256, 512, g3::LDS_BYTES, stream>>>(h, wg, wu, st, cg, hh);
        break;
    case 4:
        (void)hipFuncSetAttribute((const void*)g24_kernel<DF, false>, hipFuncAttributeMaxDynamicSharedMemorySize, g24::LDS_BYTES);
        g24_kernel<DF, false><<<256, 512, g24::LDS_BYTES, stream>>>(hh, wd, h, out, nullptr);
        break;
    }
}
constexpr int NUM_STAGES = 6;

extern "C" void kernel_launch(void* const* d_in, const int* in_sizes, int n_in, void* d_out, int out_size,
                              void* d_ws, size_t ws_size, hipStream_t stream) {
    run_stage(5, d_in, d_out, d_ws, stream);
    for (int s = 0; s < 5; ++s) run_stage(s, d_in, d_out, d_ws, stream);
}
```

```cpp
#include <hip/hip_runtime.h>
#include <hip/hip_bf16.h>
#include <stdint.h>
#include <type_traits>

#define LAS __attribute__((address_space(3)))
#define DEVI __device__ __forceinline__
#define IC(n) std::integral_constant<int, (n)>{}

typedef __attribute__((ext_vector_type(8))) __bf16 bf16x8;
typedef __attribute__((ext_vector_type(2))) __bf16 bf16x2;
typedef __attribute__((ext_vector_type(4))) short s16x4;
typedef __attribute__((ext_vector_type(8))) short s16x8;
typedef __attribute__((ext_vector_type(4))) float f32x4;
typedef __attribute__((ext_vector_type(2))) float f32x2;
typedef __attribute__((ext_vector_type(4))) unsigned u32x4;
typedef __attribute__((ext_vector_type(2))) unsigned u32x2;
typedef unsigned short u16;
typedef unsigned long long u64;
typedef __amdgpu_buffer_rsrc_t rsrc_t;

constexpr int SEQ = 2048, DM = 1024, NH = 32, DF = 3584;
constexpr float LN_EPS = 1e-5f;
constexpr int LDS_BYTES = 163840;
constexpr int SC1 = 16;

constexpr unsigned FL_EPOCH = 0;
constexpr unsigned FL_F1R = 1024;
constexpr unsigned FL_F1C = FL_F1R + 2048;
constexpr unsigned FL_F2 = FL_F1C + 2048;
constexpr unsigned FL_F3 = FL_F2 + 2048;
constexpr unsigned FL_F4 = FL_F3 + 2048;
constexpr unsigned FL_F6 = FL_F4 + 2048;
constexpr unsigned FL_F7 = FL_F6 + 2048;
#define CHPAD 256
constexpr int CHS = SEQ * 16 + CHPAD;
constexpr unsigned WS_O = 65536;
constexpr unsigned WS_HH = WS_O + 128 * CHS;
constexpr unsigned WS_KVP = WS_HH + 448 * CHS;
constexpr unsigned WS_RS1 = WS_KVP + 256 * 2 * 1056 * 4;
constexpr unsigned WS_CS1 = WS_RS1 + 16 * 16 * 128 * 8;
constexpr unsigned WS_ST2 = WS_CS1 + 16 * 16 * 192 * 4;
constexpr unsigned WS_CS3 = WS_ST2 + SEQ * 16 * 8;
constexpr unsigned WS_END = WS_CS3 + 4 * 7168 * 4;
constexpr unsigned SENT = 0xffffffffu;

DEVI unsigned pack_bf16(float lo, float hi) {
    bf16x2 v; v.x = (__bf16)lo; v.y = (__bf16)hi;
    return __builtin_bit_cast(unsigned, v);
}
DEVI float bf_lo(unsigned u) { return __uint_as_float(u << 16); }
DEVI float bf_hi(unsigned u) { return __uint_as_float(u & 0xffff0000u); }
DEVI float round_bf16(float x) { return (float)(__bf16)x; }

template <int OFF, int POL = 0> DEVI void glds(const char* sbase, unsigned voff, unsigned lds_dst) {
    if constexpr (POL == 0)
        asm volatile("s_mov_b32 m0, %2\n\ts_nop 0\n\tglobal_load_lds_dwordx4 %0, %1 offset:%3 sc1"
                     : : "v"(voff), "s"(sbase), "s"(lds_dst), "n"(OFF) : "memory");
    else if constexpr (POL == 1)
        asm volatile("s_mov_b32 m0, %2\n\ts_nop 0\n\tglobal_load_lds_dwordx4 %0, %1 offset:%3 nt"
                     : : "v"(voff), "s"(sbase), "s"(lds_dst), "n"(OFF) : "memory");
    else
        asm volatile("s_mov_b32 m0, %2\n\ts_nop 0\n\tglobal_load_lds_dwordx4 %0, %1 offset:%3"
                     : : "v"(voff), "s"(sbase), "s"(lds_dst), "n"(OFF) : "memory");
}
#define POL1 2
#define POL0 2
#define POL2 2
DEVI bf16x8 lds_b128(LAS char* p) { return *(LAS bf16x8*)p; }
DEVI bf16x8 lds_tr2(LAS char* p0, LAS char* p1) {
    s16x4 a = __builtin_amdgcn_ds_read_tr16_b64_v4i16((LAS s16x4*)p0);
    s16x4 b = __builtin_amdgcn_ds_read_tr16_b64_v4i16((LAS s16x4*)p1);
    s16x8 r = __builtin_shufflevector(a, b, 0, 1, 2, 3, 4, 5, 6, 7);
    return __builtin_bit_cast(bf16x8, r);
}
DEVI f32x4 mfma16(bf16x8 a, bf16x8 b, f32x4 c) { return __builtin_amdgcn_mfma_f32_16x16x32_bf16(a, b, c, 0, 0, 0); }
template <int N> DEVI void wait_vm() { asm volatile("s_waitcnt vmcnt(%0)" ::"n"(N) : "memory"); }
DEVI void wait_lgkm0() { asm volatile("s_waitcnt lgkmcnt(0)" ::: "memory"); }
DEVI void bar() {
    asm volatile("" ::: "memory");
    __builtin_amdgcn_s_barrier();
    asm volatile("" ::: "memory");
}
DEVI void fence() { __builtin_amdgcn_sched_barrier(0); }
DEVI float xor16_32_sum(float v) { v += __shfl_xor(v, 16); v += __shfl_xor(v, 32); return v; }
DEVI float phi_f(float t) { return (t > 0.f ? t + 1.f : __expf(t)) + 1e-6f; }
DEVI bf16x8 ones8() {
    bf16x8 o;
#pragma unroll
    for (int j = 0; j < 8; ++j) o[j] = (__bf16)1.0f;
    return o;
}
DEVI void combine8(const f32x4 (&v)[4], float& mean, float& m2) {
    const float ms = ((v[0][0] + v[0][2]) + (v[1][0] + v[1][2])) + ((v[2][0] + v[2][2]) + (v[3][0] + v[3][2]));
    mean = ms * 0.125f;
    float dd = 0.f, q = 0.f;
#pragma unroll
    for (int i = 0; i < 4; ++i) {
        const float d0 = v[i][0] - mean, d1 = v[i][2] - mean;
        dd += d0 * d0 + d1 * d1;
        q += v[i][1] + v[i][3];
    }
    m2 = q + 64.f * dd;
}
DEVI u32x4 ld128(rsrc_t rs, unsigned off) { return __builtin_amdgcn_raw_buffer_load_b128(rs, off, 0, SC1); }
DEVI void st128(rsrc_t rs, unsigned off, u32x4 v) { __builtin_amdgcn_raw_buffer_store_b128(v, rs, off, 0, SC1); }
DEVI void st64(rsrc_t rs, unsigned off, u32x2 v) { __builtin_amdgcn_raw_buffer_store_b64(v, rs, off, 0, SC1); }
DEVI void st32(rsrc_t rs, unsigned off, unsigned v) { __builtin_amdgcn_raw_buffer_store_b32(v, rs, off, 0, SC1); }
DEVI u32x2 ld64(rsrc_t rs, unsigned off) { return __builtin_amdgcn_raw_buffer_load_b64(rs, off, 0, SC1); }
DEVI unsigned ld32(rsrc_t rs, unsigned off) { return __builtin_amdgcn_raw_buffer_load_b32(rs, off, 0, SC1); }
DEVI u32x4 no_sent(f32x4 v) {
    u32x4 u = __builtin_bit_cast(u32x4, v);
#pragma unroll
    for (int j = 0; j < 4; ++j) u[j] = min(u[j], SENT - 1u);
    return u;
}
DEVI unsigned umax4(u32x4 v) { return max(max(v[0], v[1]), max(v[2], v[3])); }
DEVI bool timed_out(u64 t0) { return __builtin_amdgcn_s_memrealtime() - t0 > 1000000ull; }

DEVI void wait_flags(rsrc_t rs, unsigned off, u32x2 tag) {
    const u64 t0 = __builtin_amdgcn_s_memrealtime();
    for (;;) {
        const u32x2 v = ld64(rs, off);
        if (__all(v.x == tag.x && v.y == tag.y)) break;
        if (__builtin_amdgcn_s_memrealtime() - t0 > 1000000ull) break;
        __builtin_amdgcn_s_sleep(1);
    }
    asm volatile("" ::: "memory");
}

#define STAMP(kid, i)

struct Ctx {
    LAS char* lds;
    unsigned lds0;
    rsrc_t rs;
    rsrc_t ro;
    const char* outp;
    const char* ws;
    u32x2 tag;
    int tid, lane, w, bid;
};
template <int N> DEVI void finish_tag(Ctx& c) {
    asm volatile("s_waitcnt vmcnt(%1)" : "+v"(c.tag) : "n"(N) : "memory");
    const unsigned ex = __builtin_amdgcn_readfirstlane(c.tag.x), ey = __builtin_amdgcn_readfirstlane(c.tag.y);
    c.tag.x = ex + 1u;
    c.tag.y = ey ^ 0x9e3779b9u;
}
template <int STRIDE> DEVI void asm_load4(rsrc_t rs, unsigned voff, f32x4 (&v)[4]) {
    asm volatile("buffer_load_dwordx4 %0, %4, %5, 0 offen sc1\n\tbuffer_load_dwordx4 %1, %4, %5, %6 offen sc1\n\t"
                 "buffer_load_dwordx4 %2, %4, %5, %7 offen sc1\n\tbuffer_load_dwordx4 %3, %4, %5, %8 offen sc1"
                 : "=&v"(v[0]), "=&v"(v[1]), "=&v"(v[2]), "=&v"(v[3])
                 : "v"(voff), "s"(rs), "s"(STRIDE), "s"(2 * STRIDE), "s"(3 * STRIDE)
                 : "memory");
}
template <int N> DEVI void wait_regs4(f32x4 (&v)[4]) {
    asm volatile("s_waitcnt vmcnt(%4)" : "+v"(v[0]), "+v"(v[1]), "+v"(v[2]), "+v"(v[3]) : "n"(N) : "memory");
}

namespace g2 { constexpr int B_RING = 65536, B_PIECE = 8192; }
namespace g4 { constexpr int B_RING = 81920, B_PIECE = 16384; }
namespace g3 {
constexpr int A_PIECE = 16384, B_SUB = 32 * 224, B_PIECE = 2 * B_SUB;
constexpr int NR = 5;
constexpr int B_OFF = NR * A_PIECE;
constexpr int TAB_OFF = B_OFF + NR * B_PIECE;
constexpr int SCR_OFF = TAB_OFF + 4096 + 1024;
constexpr int OPITCH = 240;
constexpr unsigned BSTEP = 32 * DF * 2, AHALF = 128 * DM * 2;
}

DEVI unsigned inbox_of(int b) { return WS_O + (unsigned)((b & 1) * 4 + ((b >> 3) & 3)) * 4096u + (unsigned)(((b & 7) >> 1) * 8 + (b >> 5)) * (4u * CHS); }

template <bool G4> struct RsGeom {
    int pm, pn, ks;
    unsigned voffB[2];
    const char* gB;
    DEVI RsGeom(const Ctx& c, const u16* W) {
        const int xcd = c.bid & 7, loc = c.bid >> 3;
        if constexpr (G4) {
            ks = xcd & 1; pm = (xcd >> 1) * 4 + (loc & 3); pn = loc >> 2;
#pragma unroll
            for (int j = 0; j < 2; ++j) {
                const int row = 4 * (2 * c.w + j) + (c.lane >> 4), ch = c.lane & 15;
                const int s = (row & 3) | (((row >> 3) & 1) << 2);
                voffB[j] = (unsigned)(row * 2048 + pn * 256 + ((ch ^ (s << 1)) * 16) - j * 1024);
            }
            gB = (const char*)W + (size_t)ks * 1792 * 2048;
        } else {
            ks = 0; pm = (xcd & 3) * 4 + (loc & 3); pn = (xcd >> 2) * 8 + (loc >> 2);
            const int row = 8 * c.w + (c.lane >> 3), cb = c.lane & 7;
            const int f = ((row >> 1) & 1) | (((row >> 3) & 1) << 1);
            voffB[0] = (unsigned)(row * 2048 + pn * 128 + ((cb ^ (f << 1)) * 16));
            voffB[1] = 0;
            gB = (const char*)W;
        }
    }
    DEVI void pieceB(const Ctx& c, int p) const {
        const char* b = gB + (size_t)p * (64 * 2048);
        if constexpr (G4) {
            const unsigned d = c.lds0 + g4::B_RING + (p & 3) * g4::B_PIECE + 2 * c.w * 1024;
            glds<0>(b, voffB[0], d); glds<1024>(b, voffB[1], d);
        } else {
            glds<0, POL2>(b, voffB[0], c.lds0 + g2::B_RING + (p % 12) * g2::B_PIECE + c.w * 1024);
        }
    }
};

struct G3Geom {
    int pm, pn;
    unsigned voffB[2];
    const char* gB[2];
    unsigned ldsB, ringmask;
    DEVI G3Geom(const Ctx& c, const u16* wg, const u16* wu, const u16* wd) {
        using namespace g3;
        const int xcd = c.bid & 7, loc = c.bid >> 3;
        pm = (xcd & 1) * 4 + (loc & 3); pn = (xcd >> 1) * 8 + (loc >> 2);
        if (c.w < 7) {
#pragma unroll
            for (int j = 0; j < 2; ++j) {
                const int b = 2 * c.w + j, sub = b >= 7 ? 1 : 0;
                const int i = (b - 7 * sub) * 64 + c.lane, rho = i / 14, cc = i % 14;
                const int r = (rho & ~12) | ((rho & 4) << 1) | ((rho & 8) >> 1);
                voffB[j] = (unsigned)(r * DF * 2 + (pn * 112 + cc * 8) * 2);
                gB[j] = (const char*)(sub ? wu : wg) - j * 1024;
            }
            ldsB = c.lds0 + B_OFF + 2 * c.w * 1024;
            ringmask = ~0u;
        } else {
            const int l0 = min(c.lane & 7, 6);
            voffB[0] = (unsigned)((c.bid * 7 + l0) * 128);
            voffB[1] = (unsigned)((c.bid * 7 + l0) * 128 + 64);
            gB[0] = (const char*)wd; gB[1] = (const char*)wd - 1024;
            ldsB = c.lds0 + SCR_OFF;
            ringmask = 0u;
        }
    }
    DEVI void prefetchB(const Ctx& c) const {
#pragma unroll
        for (int p = 0; p < 4; ++p) {
            const unsigned d = ldsB + ((unsigned)(p * g3::B_PIECE) & ringmask);
            glds<0>(gB[0] + (size_t)p * g3::BSTEP, voffB[0], d);
            glds<1024>(gB[1] + (size_t)p * g3::BSTEP, voffB[1], d);
        }
    }
};

namespace g1 {
constexpr int A_BYTES = 16384, B_PITCH = 384, STAGE = 40960;
constexpr int TAB = 40960, CRED = 43008, KVT = 47104;
#define TCHMODE 0
#define NT0 0
#define TCH0 3
}

template <bool MEGA>
DEVI void phase1(Ctx& c, const u16* __restrict__ x, const u16* __restrict__ wqkv, const u16* __restrict__ wo,
                 const u16* __restrict__ wg, const u16* __restrict__ wu) {
    using namespace g1;
    STAMP(0, 0);
    LAS char* lds = c.lds;
    const int tid = c.tid, lane = c.lane, w = c.w;
    const int wm = w >> 1, wn = w & 1;
    const int xcd = c.bid & 7, loc = c.bid >> 3;
    const int pm = (xcd & 1) * 8 + (loc & 7);
    const int hp = (xcd >> 1) * 4 + (loc >> 3);
    const int s0 = (pm + hp) & 15;
    const int l15 = lane & 15, g = lane >> 4, q4 = (lane >> 2) & 3, p4 = lane & 3;

    unsigned voff[6], dst[6];
#pragma unroll
    for (int k = 0; k < 6; ++k) {
        const int i = w + 7 * k < 40 ? w + 7 * k : w + 7 * k - 7;
        const int rowa = 8 * i + (lane >> 3), ch = (lane & 7) ^ ((rowa >> 1) & 7);
        const int ib = (i - 16) * 64 + lane;
        const int row = ib / 24, cc = ib % 24, blk = cc >> 3, cb = cc & 7;
        const int f = ((row >> 1) & 1) | (((row >> 3) & 1) << 1);
        const unsigned oa = (unsigned)(rowa * DM * 2 + ch * 16);
        const unsigned ob = (unsigned)(row * (3 * DM) * 2 + (blk * DM + hp * 64 + (cb ^ (f << 1)) * 8) * 2);
        voff[k] = k < 2 ? oa : (k > 2 ? ob : (i < 16 ? oa : ob));
        dst[k] = c.lds0 + i * 1024;
    }
    const char* gA = (const char*)x + (size_t)pm * 128 * DM * 2;
    const char* gB = (const char*)wqkv;
    auto issue = [&](int s, int k) {
        const int ck = (s0 + s) & 15;
        const char* pa = gA + ck * 128;
        const char* pb = gB + (size_t)ck * (64 * 3 * DM * 2);
        if (s < NT0) glds<0, 1>(k < 2 ? pa : (k > 2 ? pb : (w < 2 ? pa : pb)), voff[k], dst[k] + (unsigned)((s & 3) * STAGE));
        else glds<0, POL1>(k < 2 ? pa : (k > 2 ? pb : (w < 2 ? pa : pb)), voff[k], dst[k] + (unsigned)((s & 3) * STAGE));
    };
    if (w < 7) {
#pragma unroll
        for (int s = 0; s < 4; ++s)
#pragma unroll
            for (int k = 0; k < 6; ++k) issue(s, k);
    }
    const unsigned kvslot = WS_KVP + (hp * 16 + pm) * (2 * 1056 * 4);
    {
        const u32x4 mk = {SENT, SENT, SENT, SENT};
        st128(c.rs, kvslot + tid * 16, mk);
        st128(c.rs, kvslot + 8192 + (tid & 15) * 16, mk);
    }
    fence();

    const int fB = ((q4 >> 1) & 1) | ((g & 1) << 1);
    int va[4][2], vb[4][2];
#pragma unroll
    for (int s = 0; s < 4; ++s)
#pragma unroll
        for (int i = 0; i < 2; ++i) {
            va[s][i] = s * STAGE + (32 * wm + l15) * 128 + 16 * ((4 * i + g) ^ (l15 >> 1));
            vb[s][i] = s * STAGE + A_BYTES + (8 * g + q4) * B_PITCH + 32 * ((2 * wn + i) ^ fB) + 8 * p4;
        }
    f32x4 acc[2][6];
#pragma unroll
    for (int m = 0; m < 2; ++m)
#pragma unroll
        for (int n = 0; n < 6; ++n) acc[m][n] = (f32x4){0.f, 0.f, 0.f, 0.f};

    const unsigned floff = lane < 16 ? FL_F1R + pm * 128 + l15 * 8 : FL_F1C + hp * 128 + l15 * 8;
    const unsigned vlin = (unsigned)lane * 16u;
    auto issue_stats = [&](int k) {
        const int i = w + 7 * k;
        glds<0>(c.ws + (i < 16 ? WS_RS1 + pm * 16384 + i * 1024 : WS_CS1 + hp * 12288 + (i - 16) * 1024), vlin, dst[k]);
    };
    const int cid = c.bid < 224 ? c.bid : c.bid - 32, cg = cid % 56, rr = cid / 56;
    const char* gC = (const char*)(cg < 28 ? wg : wu) + (size_t)(rr * 256 + w * 32) * (DF * 2) + (cg < 28 ? cg : cg - 28) * 256;
    const unsigned voffC = (unsigned)(g * (DF * 2) + l15 * 16);
    u32x4 cv[8];
    unsigned touch = 0;
    const char* gT = (const char*)(c.bid < 128 ? wg : wu) + (size_t)(c.bid & 127) * 57344;
    const RsGeom<false> gn(c, wo);

    LAS f32x2* rowtab = (LAS f32x2*)(lds + TAB);
    LAS float* coltab = (LAS float*)(lds + TAB + 1024);
    LAS unsigned* verdict = (LAS unsigned*)(lds + TAB + 1792);
    auto tables = [&]() {
        if (tid < 256) {
            const int row = tid >> 1, hf = tid & 1;
            f32x4 v[4];
#pragma unroll
            for (int i = 0; i < 4; ++i) {
                const f32x2 p0 = *(LAS f32x2*)(lds + ((8 * hf + 2 * i) * 128 + row) * 8);
                const f32x2 p1 = *(LAS f32x2*)(lds + ((8 * hf + 2 * i + 1) * 128 + row) * 8);
                v[i] = (f32x4){p0.x, p0.y, p1.x, p1.y};
            }
            float mean, m2;
            combine8(v, mean, m2);
            const float mo = __shfl_xor(mean, 1), m2o = __shfl_xor(m2, 1);
            const float d = mean - mo;
            const float var = (m2 + m2o + 256.f * d * d) * (1.f / DM);
            if (hf == 0) rowtab[row] = (f32x2){0.5f * (mean + mo), rsqrtf(var + LN_EPS)};
        } else if (tid < 448) {
            const int col = tid - 256;
            float s = 0.f;
#pragma unroll
            for (int i = 0; i < 16; ++i) s += *(LAS float*)(lds + A_BYTES + (i * 192 + col) * 4);
            coltab[col] = s;
        }
    };

    bf16x8 fa[2][2], fb[2][6];
    auto ldA = [&](int s, int kk, int m) { return lds_b128(lds + va[s][kk] + m * 2048); };
    auto ldB = [&](int s, int kk, int n) {
        LAS char* pb = lds + vb[s][n & 1] + kk * 32 * B_PITCH + (n >> 1) * 128;
        return lds_tr2(pb, pb + 4 * B_PITCH);
    };
    auto mainloop = [&](auto ld_tag) {
        constexpr bool LOADER = decltype(ld_tag)::value;
        u32x2 fl;
        auto vm_slot = [&](auto t_tag, int k) {
            constexpr int T = decltype(t_tag)::value;
            if constexpr (LOADER) {
                if constexpr (T == 9) {
                    if (k == 0) asm volatile("buffer_load_dwordx2 %0, %1, %2, 0 offen sc1" : "=v"(fl) : "v"(floff), "s"(c.rs) : "memory");
                }
                if constexpr (T >= 0 && T < 12) issue(T + 4, k);
                if constexpr (T == 12) { if (k < 4) issue_stats(k); }
            } else {
                if (k == 0) {
                    if constexpr (T == 2) {
                        asm volatile("global_load_dword %0, %1, %2" : "+v"(touch) : "v"(8 * vlin), "s"((const char*)wo + c.bid * 8192) : "memory");
                        asm volatile("global_load_dword %0, %1, %2" : "+v"(touch) : "v"(0u), "s"(c.ws + c.bid * 128) : "memory");
                    }
                    if constexpr (T >= TCH0 && T < TCH0 + 7)
                        asm volatile("global_load_dword %0, %1, %2" : "+v"(touch) : "v"(8 * vlin), "s"(gT + (T - TCH0) * 8192) : "memory");
                }
            }
            if constexpr (T == 13 && MEGA) { if (k < 2) gn.pieceB(c, k); }
            if constexpr (T == 14 && MEGA) { if (k < 5) gn.pieceB(c, 2 + k); }
            if constexpr (T == 15 && MEGA) { gn.pieceB(c, 7 + 2 * k); if (k < 2) gn.pieceB(c, 8 + 2 * k); }
        };
        auto half = [&](auto t_tag, auto kk_tag) {
            constexpr int t = decltype(t_tag)::value, kk = decltype(kk_tag)::value;
            constexpr int nt = kk ? t + 1 : t, nk = kk ^ 1, ns = nt & 3;
#pragma unroll
            for (int i = 0; i < 12; ++i) {
                const int m = i / 6, n = i % 6;
                if (n < 2) acc[m][n] = mfma16(fb[kk][n], fa[kk][m], acc[m][n]);
                else acc[m][n] = mfma16(fa[kk][m], fb[kk][n], acc[m][n]);
                if constexpr (nt < 16) {
                    if (i == 0) { fa[nk][0] = ldA(ns, nk, 0); fa[nk][1] = ldA(ns, nk, 1); }
                    else if (i <= 6) fb[nk][i - 1] = ldB(ns, nk, i - 1);
                }
                fence();
                if (kk == 1 && (i & 3) == 3) vm_slot(IC(t), i >> 2);
                if (kk == 0 && (i & 3) == 3) vm_slot(IC(t - 1), 3 + (i >> 2));
                fence();
            }
        };
        auto step = [&](auto t_tag, auto wv_tag) {
            constexpr int t = decltype(t_tag)::value, WV = decltype(wv_tag)::value;
            half(t_tag, IC(0));
            if constexpr (LOADER) { if constexpr (WV >= 0) wait_vm<WV>(); }
            else if constexpr (t == 2) wait_vm<0>();
            wait_lgkm0();
            fence();
            bar();
            fence();
            if constexpr (LOADER && t == 3) {
                if (tid < 2) st64(c.rs, tid ? FL_F1C + hp * 128 + pm * 8 : FL_F1R + pm * 128 + hp * 8, c.tag);
                fence();
            }
            if constexpr (t == 13) {
                bool ok = true;
                if constexpr (LOADER) {
                    asm volatile("" : "+v"(fl));
                    ok = __all(fl.x == c.tag.x && fl.y == c.tag.y);
                }
                if (lane == 0) verdict[w] = ok ? 0u : 1u;
                fence();
            }
            if constexpr (LOADER && t == 14) { tables(); fence(); }
            half(t_tag, IC(1));
        };
        step(IC(0), IC(16)); step(IC(1), IC(16)); step(IC(2), IC(16)); step(IC(3), IC(12));
        step(IC(4), IC(12)); step(IC(5), IC(12)); step(IC(6), IC(12)); step(IC(7), IC(12));
        step(IC(8), IC(12)); step(IC(9), IC(12)); step(IC(10), IC(12)); step(IC(11), IC(12));
        step(IC(12), IC(12)); step(IC(13), IC(10)); step(IC(14), IC(MEGA ? 2 : 0)); step(IC(15), IC(-1));
    };

    if (w < 7) wait_vm<20>(); else wait_vm<2>();
    finish_tag<26>(c);
    bar();
    STAMP(0, 1);
    fa[0][0] = ldA(0, 0, 0); fa[0][1] = ldA(0, 0, 1);
#pragma unroll
    for (int n = 0; n < 6; ++n) fb[0][n] = ldB(0, 0, n);
    {
        const int row = 16 * w + l15;
        const u32x4 r0 = __builtin_bit_cast(u32x4, lds_b128(lds + row * 128 + 16 * (g ^ (l15 >> 1))));
        const u32x4 r1 = __builtin_bit_cast(u32x4, lds_b128(lds + row * 128 + 16 * ((4 + g) ^ (l15 >> 1))));
        float v[16];
#pragma unroll
        for (int j = 0; j < 4; ++j) { v[2 * j] = bf_lo(r0[j]); v[2 * j + 1] = bf_hi(r0[j]); v[8 + 2 * j] = bf_lo(r1[j]); v[9 + 2 * j] = bf_hi(r1[j]); }
        float s = 0.f;
#pragma unroll
        for (int j = 0; j < 16; ++j) s += v[j];
        const float mu = xor16_32_sum(s) * (1.f / 64.f);
        float m2 = 0.f;
#pragma unroll
        for (int j = 0; j < 16; ++j) { const float d = v[j] - mu; m2 += d * d; }
        m2 = xor16_32_sum(m2);
        const int part = wm == 3 ? 0 : wm;
        f32x4 cs[2];
#pragma unroll
        for (int i = 0; i < 2; ++i) {
            cs[i] = (f32x4){0.f, 0.f, 0.f, 0.f};
#pragma unroll
            for (int kh = 0; kh < 2; ++kh) {
                LAS char* pb = lds + vb[0][i] + kh * 32 * B_PITCH + part * 128;
                cs[i] = mfma16(ones8(), lds_tr2(pb, pb + 4 * B_PITCH), cs[i]);
            }
        }
        st64(c.rs, WS_RS1 + ((pm * 16 + s0) * 128 + row) * 8, (u32x2){__float_as_uint(mu), __float_as_uint(m2)});
        st32(c.rs, WS_CS1 + ((hp * 16 + s0) * 192 + part * 64 + wn * 32 + (g & 1) * 16 + l15) * 4, __float_as_uint((g & 1) ? cs[1][0] : cs[0][0]));
    }
    fence();
    if (w < 7) mainloop(std::true_type{}); else mainloop(std::false_type{});
    STAMP(0, 2);
    {
        const u32x4 v0 = *(LAS u32x4*)verdict, v1 = *(LAS u32x4*)(verdict + 4);
        if (__builtin_amdgcn_readfirstlane(umax4(v0) | umax4(v1))) {
            wait_flags(c.rs, floff, c.tag);
            if (w < 7) {
#pragma unroll
                for (int j = 0; j < 4; ++j) issue_stats(j);
            }
            wait_vm<0>();
            bar();
            tables();
            wait_lgkm0();
            bar();
        }
    }
    const int rbase = 32 * wm;
    bf16x8 pk[2], pv[2];
    {
        f32x2 rt[2][4];
#pragma unroll
        for (int mm = 0; mm < 2; ++mm)
#pragma unroll
            for (int j = 0; j < 4; ++j) rt[mm][j] = rowtab[rbase + 16 * mm + 4 * g + j];
#pragma unroll
        for (int i = 0; i < 2; ++i) {
            const float cwk = coltab[64 + wn * 32 + i * 16 + l15], cwv = coltab[128 + wn * 32 + i * 16 + l15];
#pragma unroll
            for (int mm = 0; mm < 2; ++mm)
#pragma unroll
                for (int j = 0; j < 4; ++j) {
                    const float kk = round_bf16(rt[mm][j].y * (acc[mm][2 + i][j] - rt[mm][j].x * cwk));
                    const float vv = rt[mm][j].y * (acc[mm][4 + i][j] - rt[mm][j].x * cwv);
                    pk[i][mm * 4 + j] = (__bf16)phi_f(kk);
                    pv[i][mm * 4 + j] = (__bf16)vv;
                }
        }
    }
    LAS float* red = (LAS float*)lds;
    {
        LAS float* mine = red + w * 1056;
#pragma unroll
        for (int i = 0; i < 2; ++i) {
#pragma unroll
            for (int i2 = 0; i2 < 2; ++i2) {
                const f32x4 d = mfma16(pk[i], pv[i2], (f32x4){0.f, 0.f, 0.f, 0.f});
                *(LAS f32x4*)(mine + (16 * i2 + l15) * 32 + 16 * i + 4 * g) = d;
            }
            const f32x4 z = mfma16(pk[i], ones8(), (f32x4){0.f, 0.f, 0.f, 0.f});
            if (l15 == 0) *(LAS f32x4*)(mine + 1024 + 16 * i + 4 * g) = z;
        }
    }
    wait_lgkm0();
    bar();
    STAMP(0, 3);
    const int hh = tid >> 8, e4 = tid & 255;
    {
        f32x4 s = *(LAS f32x4*)(red + (hh + 0) * 1056 + e4 * 4);
        s += *(LAS f32x4*)(red + (hh + 2) * 1056 + e4 * 4);
        s += *(LAS f32x4*)(red + (hh + 4) * 1056 + e4 * 4);
        s += *(LAS f32x4*)(red + (hh + 6) * 1056 + e4 * 4);
        st128(c.rs, kvslot + tid * 16, no_sent(s));
        if (tid < 16) {
            const int h2 = tid >> 3, z4 = 1024 + (tid & 7) * 4;
            f32x4 z = *(LAS f32x4*)(red + (h2 + 0) * 1056 + z4);
            z += *(LAS f32x4*)(red + (h2 + 2) * 1056 + z4);
            z += *(LAS f32x4*)(red + (h2 + 4) * 1056 + z4);
            z += *(LAS f32x4*)(red + (h2 + 6) * 1056 + z4);
            st128(c.rs, kvslot + 8192 + tid * 16, no_sent(z));
        }
    }
    fence();
#pragma unroll
    for (int j = 0; j < 8; ++j)
        asm volatile("global_load_dwordx4 %0, %1, %2" : "=v"(cv[j]) : "v"(voffC), "s"(gC + (size_t)j * (4 * DF * 2)) : "memory");
    fence();
    bf16x8 pq[2];
#pragma unroll
    for (int mm = 0; mm < 2; ++mm) {
        const f32x2 rt = rowtab[rbase + 16 * mm + l15];
#pragma unroll
        for (int i = 0; i < 2; ++i) {
            const f32x4 cw = *(LAS f32x4*)(coltab + wn * 32 + i * 16 + 4 * g);
#pragma unroll
            for (int j = 0; j < 4; ++j) pq[mm][4 * i + j] = (__bf16)phi_f(round_bf16(rt.y * (acc[mm][i][j] - rt.x * cw[j])));
        }
    }
    asm volatile("" : "+v"(pq[0]), "+v"(pq[1]));
    STAMP(0, 4);
    LAS u16* kvt = (LAS u16*)(lds + KVT);
    {
        const unsigned src = WS_KVP + hp * 16 * (2 * 1056 * 4);
        const u64 t0 = __builtin_amdgcn_s_memrealtime();
        for (;;) {
            const unsigned v = ld32(c.rs, src + l15 * (2 * 1056 * 4) + w * 1024);
            if (__all(v != SENT) || timed_out(t0)) break;
            __builtin_amdgcn_s_sleep(1);
        }
        STAMP(0, 5);
    {
        asm volatile("s_waitcnt vmcnt(0)"
                     : "+v"(cv[0]), "+v"(cv[1]), "+v"(cv[2]), "+v"(cv[3]), "+v"(cv[4]), "+v"(cv[5]), "+v"(cv[6]), "+v"(cv[7])
                     : : "memory");
        asm volatile("" : "+v"(touch));
        u32x4 sel;
#pragma unroll
        for (int j = 0; j < 4; ++j) sel[j] = (l15 >> 1) == j ? ((l15 & 1) ? 0x3f800000u : 0x00003f80u) : 0u;
        f32x4 d = {0.f, 0.f, 0.f, 0.f};
#pragma unroll
        for (int j = 0; j < 8; ++j) d = mfma16(__builtin_bit_cast(bf16x8, sel), __builtin_bit_cast(bf16x8, cv[j]), d);
        if (g < 2) *(LAS f32x4*)(lds + CRED + w * 512 + (8 * l15 + 4 * g) * 4) = d;
    }
        auto gather = [&](auto z_tag) {
            constexpr bool Z = decltype(z_tag)::value;
            f32x4 s, z;
            for (;;) {
                u32x4 v[16], vz[4];
#pragma unroll
                for (int p = 0; p < 16; ++p) v[p] = ld128(c.rs, src + p * (2 * 1056 * 4) + tid * 16);
                if constexpr (Z) {
#pragma unroll
                    for (int p = 0; p < 4; ++p) vz[p] = ld128(c.rs, src + (4 * g + p) * (2 * 1056 * 4) + 8192 + l15 * 16);
                }
                s = (f32x4){0.f, 0.f, 0.f, 0.f}; z = s;
#pragma unroll
                for (int p = 0; p < 16; ++p) s += __builtin_bit_cast(f32x4, v[p]);
                if constexpr (Z) {
#pragma unroll
                    for (int p = 0; p < 4; ++p) z += __builtin_bit_cast(f32x4, vz[p]);
                }
                const f32x4 t = s + z;
                if (!__any(t[0] != t[0] || t[1] != t[1] || t[2] != t[2] || t[3] != t[3])) break;
                unsigned mx = 0;
#pragma unroll
                for (int p = 0; p < 16; ++p) mx = max(mx, umax4(v[p]));
                if constexpr (Z) {
#pragma unroll
                    for (int p = 0; p < 4; ++p) mx = max(mx, umax4(vz[p]));
                }
                if (!__any(mx == SENT) || timed_out(t0)) break;
            }
            *(LAS u32x2*)(kvt + hh * 1056 + e4 * 4) = (u32x2){pack_bf16(s[0], s[1]), pack_bf16(s[2], s[3])};
            if constexpr (Z) {
#pragma unroll
                for (int j = 0; j < 4; ++j) z[j] = xor16_32_sum(z[j]);
                if (lane < 16) *(LAS u32x2*)(kvt + (lane >> 3) * 1056 + 1024 + (lane & 7) * 4) = (u32x2){pack_bf16(z[0], z[1]), pack_bf16(z[2], z[3])};
            }
        };
        if (w == 1) gather(std::true_type{}); else gather(std::false_type{});
        wait_lgkm0();
        bar();
        STAMP(0, 6);
        LAS u16* kh = kvt + wn * 1056;
        const int e0 = 8 * (l15 >> 2) + (l15 & 3);
        auto rd8 = [&](LAS u16* p) {
            const u32x2 lo = *(LAS u32x2*)(p + 4 * g), hi = *(LAS u32x2*)(p + 16 + 4 * g);
            return __builtin_bit_cast(bf16x8, (u32x4){lo.x, lo.y, hi.x, hi.y});
        };
        const bf16x8 a0 = rd8(kh + e0 * 32), a1 = rd8(kh + (e0 + 4) * 32), az = rd8(kh + 1024);
        const f32x4 zero = {0.f, 0.f, 0.f, 0.f};
#pragma unroll
        for (int mm = 0; mm < 2; ++mm) {
            const f32x4 n0 = mfma16(a0, pq[mm], zero), n1 = mfma16(a1, pq[mm], zero), dn = mfma16(az, pq[mm], zero);
            const float inv = 1.f / (dn[0] + 1e-6f);
            u32x4 r;
            r.x = pack_bf16(n0[0] * inv, n0[1] * inv); r.y = pack_bf16(n0[2] * inv, n0[3] * inv);
            r.z = pack_bf16(n1[0] * inv, n1[1] * inv); r.w = pack_bf16(n1[2] * inv, n1[3] * inv);
            st128(c.rs, WS_O + (hp * 8 + wn * 4 + g) * CHS + (pm * 128 + rbase + 16 * mm + l15) * 16, r);
        }
        if (tid >= 480) {
            const int q = tid - 480;
            f32x4 s = {0.f, 0.f, 0.f, 0.f};
#pragma unroll
            for (int j = 0; j < 8; ++j) s += *(LAS f32x4*)(lds + CRED + j * 512 + q * 16);
            st128(c.rs, WS_CS3 + (rr * 7168 + cg * 128 + q * 4) * 4, __builtin_bit_cast(u32x4, s));
        }
    }
    wait_vm<0>();
    bar();
    if (tid == 0) st64(c.rs, FL_F3 + pm * 128 + hp * 8, c.tag);
    STAMP(0, 7);
}

template <bool MEGA>
DEVI void phase2(const Ctx& c, const u16* __restrict__ wo, const u16* __restrict__ x,
                 const u16* __restrict__ wg, const u16* __restrict__ wu, const u16* __restrict__ wd) {
    using namespace g2;
    STAMP(2, 0);
    LAS char* lds = c.lds;
    const int tid = c.tid, lane = c.lane, w = c.w;
    const int wm = w >> 1, wn = w & 1;
    const int l15 = lane & 15, g = lane >> 4, q4 = (lane >> 2) & 3, p4 = lane & 3;
    const RsGeom<false> ge(c, wo);
    const int pm = ge.pm, pn = ge.pn;

    unsigned voffA[2];
#pragma unroll
    for (int j = 0; j < 2; ++j) {
        voffA[j] = (unsigned)((lane >> 3) * CHS + (8 * (2 * w + j) + (lane & 7)) * 16 + (1 - j) * 1024);
    }
    const char* gA = c.ws + WS_O + pm * 128 * 16 - 1024;
    const unsigned dA = c.lds0 + 2 * w * 1024;
    auto issueA = [&](int p, int j) {
        if (j == 0) glds<0>(gA + p * (8 * CHS), voffA[0], dA + (p & 3) * 16384);
        else glds<1024>(gA + p * (8 * CHS), voffA[1], dA + (p & 3) * 16384);
    };
    const int fB = ((q4 >> 1) & 1) | ((g & 1) << 1);
    int va[2], vb[2][2];
#pragma unroll
    for (int i = 0; i < 2; ++i) {
        va[i] = (4 * wm + (l15 >> 3)) * 1024 + (4 * i + g) * 128 + (l15 & 7) * 16;
        vb[0][i] = B_RING + (8 * g + q4) * 128 + 32 * ((2 * wn + i) ^ fB) + 8 * p4;
        vb[1][i] = vb[0][i] + 6 * B_PIECE;
    }
    const unsigned voffX = (unsigned)((16 * w + (lane >> 3)) * DM * 2 + (tid & 7) * 16);
    const char* gX = (const char*)x + ((size_t)pm * 128 * DM + pn * 64) * 2;
    if constexpr (!MEGA) {
#pragma unroll
        for (int p = 0; p < 12; ++p) ge.pieceB(c, p);
        wait_vm<0>();
    }
    fence();
    if (w == 0) wait_flags(c.rs, FL_F3 + pm * 128 + l15 * 8, c.tag);
    bar();
    STAMP(2, 1);
#pragma unroll
    for (int p = 0; p < 4; ++p) { issueA(p, 0); issueA(p, 1); }
    u32x4 xr[2];
    asm volatile("global_load_dwordx4 %0, %1, %2" : "=v"(xr[0]) : "v"(voffX), "s"(gX) : "memory");
    asm volatile("global_load_dwordx4 %0, %1, %2" : "=v"(xr[1]) : "v"(voffX), "s"(gX + 8 * DM * 2) : "memory");

    f32x4 acc[2][2];
#pragma unroll
    for (int m = 0; m < 2; ++m)
#pragma unroll
        for (int n = 0; n < 2; ++n) acc[m][n] = (f32x4){0.f, 0.f, 0.f, 0.f};
    bf16x8 fa[2][2], fb[2][2];
    auto ldA = [&](int p, int kk, int m) { return lds_b128(lds + va[kk] + (p & 3) * 16384 + m * 2048); };
    auto ldB = [&](int p, int kk, int n) {
        const int s = p % 12;
        LAS char* pb = lds + vb[s / 6][n] + (s % 6) * B_PIECE + kk * 4096;
        return lds_tr2(pb, pb + 512);
    };
    auto half = [&](auto t_tag, auto kk_tag) {
        constexpr int t = decltype(t_tag)::value, kk = decltype(kk_tag)::value;
        constexpr int nt = kk ? t + 1 : t, nk = kk ^ 1;
#pragma unroll
        for (int i = 0; i < 4; ++i) {
            const int m = i >> 1, n = i & 1;
            if constexpr (nt < 16) {
                if (i == 0) { fa[nk][0] = ldA(nt, nk, 0); fa[nk][1] = ldA(nt, nk, 1); fb[nk][0] = ldB(nt, nk, 0); }
                if (i == 1) fb[nk][1] = ldB(nt, nk, 1);
            }
            acc[m][n] = mfma16(fb[kk][n], fa[kk][m], acc[m][n]);
            fence();
            if constexpr (kk == 1 && t + 4 < 16) {
                if (i == 1 || i == 2) issueA(t + 4, i - 1);
                if (i == 3 && t < 4) ge.pieceB(c, 12 + t);
            }
            fence();
        }
    };
    auto step = [&](auto t_tag, auto wv_tag) {
        constexpr int WV = decltype(wv_tag)::value;
        half(t_tag, IC(0));
        if constexpr (WV >= 0) {
            wait_vm<WV>();
            wait_lgkm0();
            fence();
            bar();
            fence();
        }
        half(t_tag, IC(1));
    };
    wait_vm<8>();
    bar();
    fa[0][0] = ldA(0, 0, 0); fa[0][1] = ldA(0, 0, 1); fb[0][0] = ldB(0, 0, 0); fb[0][1] = ldB(0, 0, 1);
    fence();
    step(IC(0), IC(6)); step(IC(1), IC(7)); step(IC(2), IC(8)); step(IC(3), IC(7));
    step(IC(4), IC(7)); step(IC(5), IC(6)); step(IC(6), IC(5)); step(IC(7), IC(4));
    step(IC(8), IC(4)); step(IC(9), IC(4)); step(IC(10), IC(4)); step(IC(11), IC(4));
    step(IC(12), IC(4)); step(IC(13), IC(2)); step(IC(14), IC(0)); step(IC(15), IC(-1));
    STAMP(2, 2);
    asm volatile("" : "+v"(xr[0]), "+v"(xr[1]));

    constexpr int OP = 144;
#pragma unroll
    for (int m = 0; m < 2; ++m)
#pragma unroll
        for (int n = 0; n < 2; ++n)
            *(LAS u32x2*)(lds + (32 * wm + 16 * m + l15) * OP + (32 * wn + 16 * n + 4 * g) * 2) =
                (u32x2){pack_bf16(acc[m][n][0], acc[m][n][1]), pack_bf16(acc[m][n][2], acc[m][n][3])};
    wait_lgkm0();
    bar();
    STAMP(2, 3);
    u32x2 stat = {0u, 0u};
#pragma unroll
    for (int i = 0; i < 2; ++i) {
        const int r = 16 * w + 8 * i + (lane >> 3), row = pm * 128 + r;
        const u32x4 av = *(LAS u32x4*)(lds + r * OP + (tid & 7) * 16);
        float hv[8];
#pragma unroll
        for (int j = 0; j < 4; ++j) {
            hv[2 * j] = round_bf16(bf_lo(xr[i][j]) + bf_lo(av[j]));
            hv[2 * j + 1] = round_bf16(bf_hi(xr[i][j]) + bf_hi(av[j]));
        }
        st128(c.ro, (row * DM + pn * 64 + (tid & 7) * 8) * 2,
              (u32x4){pack_bf16(hv[0], hv[1]), pack_bf16(hv[2], hv[3]), pack_bf16(hv[4], hv[5]), pack_bf16(hv[6], hv[7])});
        float s = ((hv[0] + hv[1]) + (hv[2] + hv[3])) + ((hv[4] + hv[5]) + (hv[6] + hv[7]));
        s += __shfl_xor(s, 1); s += __shfl_xor(s, 2); s += __shfl_xor(s, 4);
        const float mu = s * (1.f / 64.f);
        float m2 = 0.f;
#pragma unroll
        for (int j = 0; j < 8; ++j) { const float d = hv[j] - mu; m2 += d * d; }
        m2 += __shfl_xor(m2, 1); m2 += __shfl_xor(m2, 2); m2 += __shfl_xor(m2, 4);
        if ((lane & 1) == i) stat = (u32x2){__float_as_uint(mu), __float_as_uint(m2)};
    }
    if ((lane & 7) < 2) st64(c.rs, WS_ST2 + (pn * SEQ + pm * 128 + 16 * w + 8 * (lane & 1) + (lane >> 3)) * 8, stat);
    STAMP(2, 4);
    if (MEGA && w != 0) {
        const G3Geom gn(c, wg, wu, wd);
        gn.prefetchB(c);
        wait_vm<8>();
    } else {
        wait_vm<0>();
    }
    bar();
    if (tid == 0) st64(c.rs, FL_F4 + pm * 128 + pn * 8, c.tag);
    STAMP(2, 7);
}

template <bool MEGA>
DEVI void phase4(const Ctx& c, const u16* __restrict__ wd, u16* __restrict__ out) {
    using namespace g4;
    STAMP(4, 0);
    LAS char* lds = c.lds;
    const int tid = c.tid, lane = c.lane, w = c.w;
    const int wm = w >> 1, wn = w & 1;
    const int l15 = lane & 15, g = lane >> 4, q4 = (lane >> 2) & 3, p4 = lane & 3;
    const RsGeom<true> ge(c, wd);
    const int pm = ge.pm, pn = ge.pn, ks = ge.ks;

    unsigned voffA[2];
#pragma unroll
    for (int j = 0; j < 2; ++j) {
        voffA[j] = (unsigned)((lane >> 3) * CHS + (8 * (2 * w + j) + (lane & 7)) * 16 + (1 - j) * 1024);
    }
    constexpr int ASTEP = 8 * CHS;
    const char* gA = c.ws + WS_HH + (size_t)ks * 28 * ASTEP + pm * 128 * 16 - 1024;
    const unsigned dA = c.lds0 + 2 * w * 1024, dB = c.lds0 + B_RING + 2 * w * 1024;
    int va[2][2], vb[4];
#pragma unroll
    for (int kk = 0; kk < 2; ++kk)
#pragma unroll
        for (int m = 0; m < 2; ++m) va[kk][m] = (4 * wm + 2 * (m ^ ks) + (l15 >> 3)) * 1024 + (4 * kk + g) * 128 + (l15 & 7) * 16;
    {
        const int sB = q4 | ((g & 1) << 2);
#pragma unroll
        for (int n = 0; n < 4; ++n) vb[n] = B_RING + (8 * g + q4) * 256 + 32 * ((4 * wn + n) ^ sB) + 8 * p4;
    }
    if constexpr (!MEGA) {
#pragma unroll
        for (int p = 0; p < 4; ++p) ge.pieceB(c, p);
        wait_vm<0>();
    }
    fence();
    if (w == 0) {
        const int pb = c.bid ^ 1, ppm = (pb & 1) * 4 + ((pb >> 3) & 3), ppn = ((pb & 7) >> 1) * 8 + (pb >> 5);
        wait_flags(c.rs, lane < 16 ? FL_F6 + (pm >> 1) * 256 + ks * 128 + l15 * 8 : FL_F6 + ppm * 256 + ppn * 8, c.tag);
    }
    bar();
    STAMP(4, 1);
#pragma unroll
    for (int p = 0; p < 4; ++p) { glds<0>(gA + p * ASTEP, voffA[0], dA + p * 16384); glds<1024>(gA + p * ASTEP, voffA[1], dA + p * 16384); }

    f32x4 acc[2][4];
#pragma unroll
    for (int m = 0; m < 2; ++m)
#pragma unroll
        for (int n = 0; n < 4; ++n) acc[m][n] = (f32x4){0.f, 0.f, 0.f, 0.f};
    bf16x8 fa[2][2], fb[2][4];
    auto ldA = [&](int s, int kk, int m) { return lds_b128(lds + va[kk][m] + s * 16384); };
    auto ldB = [&](int s, int kk, int n) {
        LAS char* pb = lds + vb[n] + s * B_PIECE + kk * 8192;
        return lds_tr2(pb, pb + 1024);
    };
    u32x4 hr[2];
    const unsigned voffH = (unsigned)(((pm * 128 + 32 * (tid >> 8) + 16 * ks + ((tid >> 4) & 15)) * DM + pn * 128 + (tid & 15) * 8) * 2);
    const char* pa = gA + 4 * ASTEP;
    const char* pb = ge.gB + (size_t)4 * (64 * 2048);
    auto half = [&](auto s_tag, auto kk_tag, auto iss_tag, auto last_tag) {
        constexpr int s = decltype(s_tag)::value, kk = decltype(kk_tag)::value, ISS = decltype(iss_tag)::value;
        constexpr bool LAST = decltype(last_tag)::value;
        constexpr int ns = kk ? (s + 1) & 3 : s, nk = kk ^ 1;
#pragma unroll
        for (int i = 0; i < 8; ++i) {
            const int m = i >> 2, n = i & 3;
            acc[m][n] = mfma16(fb[kk][n], fa[kk][m], acc[m][n]);
            if constexpr (!LAST) {
                if (i == 0) { fa[nk][0] = ldA(ns, nk, 0); fa[nk][1] = ldA(ns, nk, 1); }
                else if (i <= 4) fb[nk][i - 1] = ldB(ns, nk, i - 1);
            }
            fence();
            if constexpr (kk == 1 && ISS == 1) {
                if (i == 1) glds<0>(pa + s * ASTEP, voffA[0], dA + s * 16384);
                if (i == 3) glds<1024>(pa + s * ASTEP, voffA[1], dA + s * 16384);
                if (i == 5) glds<0>(pb + (size_t)s * (64 * 2048), ge.voffB[0], dB + s * B_PIECE);
                if (i == 7) glds<1024>(pb + (size_t)s * (64 * 2048), ge.voffB[1], dB + s * B_PIECE);
            }
            if constexpr (kk == 1 && ISS == 2) {
                if (i == 1) asm volatile("buffer_load_dwordx4 %0, %1, %2, 0 offen sc1" : "=v"(hr[0]) : "v"(voffH), "s"(c.ro) : "memory");
                if (i == 3) asm volatile("buffer_load_dwordx4 %0, %1, %2, %3 offen sc1" : "=v"(hr[1]) : "v"(voffH), "s"(c.ro), "s"(64 * DM * 2) : "memory");
            }
            fence();
        }
    };
    auto step = [&](auto s_tag, auto iss_tag, auto wv_tag) {
        constexpr int WV = decltype(wv_tag)::value;
        half(s_tag, IC(0), iss_tag, std::false_type{});
        if constexpr (WV >= 0) {
            wait_vm<WV>();
            wait_lgkm0();
            fence();
            bar();
            fence();
        }
        half(s_tag, IC(1), iss_tag, std::integral_constant<bool, (WV < 0)>{});
    };
    wait_vm<6>();
    bar();
    fa[0][0] = ldA(0, 0, 0); fa[0][1] = ldA(0, 0, 1);
#pragma unroll
    for (int n = 0; n < 4; ++n) fb[0][n] = ldB(0, 0, n);
    fence();
    step(IC(0), IC(1), IC(4)); step(IC(1), IC(1), IC(6)); step(IC(2), IC(1), IC(8)); step(IC(3), IC(1), IC(8));
    for (int it = 0; it < 5; ++it) {
        pa += 4 * ASTEP; pb += (size_t)4 * (64 * 2048);
        step(IC(0), IC(1), IC(8)); step(IC(1), IC(1), IC(8)); step(IC(2), IC(1), IC(8)); step(IC(3), IC(1), IC(8));
    }
    step(IC(0), IC(0), IC(8)); step(IC(1), IC(0), IC(4)); step(IC(2), IC(2), IC(0)); step(IC(3), IC(0), IC(-1));
    STAMP(4, 2);

    constexpr int OP = 272;
    const unsigned xs = inbox_of(c.bid ^ 1) + (w >> 1) * CHS + (w & 1) * 2048 + lane * 16;
#pragma unroll
    for (int n = 0; n < 2; ++n) {
        u32x4 v = {pack_bf16(acc[1][2 * n][0], acc[1][2 * n][1]), pack_bf16(acc[1][2 * n][2], acc[1][2 * n][3]),
                   pack_bf16(acc[1][2 * n + 1][0], acc[1][2 * n + 1][1]), pack_bf16(acc[1][2 * n + 1][2], acc[1][2 * n + 1][3])};
        st128(c.rs, xs + n * 1024, no_sent(__builtin_bit_cast(f32x4, v)));
    }
    const unsigned xr = inbox_of(c.bid) + (w >> 1) * CHS + (w & 1) * 2048 + lane * 16;
    u32x4 pv[2];
    {
        const u64 t0 = __builtin_amdgcn_s_memrealtime();
        for (;;) {
#pragma unroll
            for (int n = 0; n < 2; ++n) pv[n] = ld128(c.rs, xr + n * 1024);
            const unsigned mx = max(umax4(pv[0]), umax4(pv[1]));
            if (!__any(mx == SENT) || timed_out(t0)) break;
            __builtin_amdgcn_s_sleep(1);
        }
    }
    STAMP(4, 3);
    asm volatile("" : "+v"(hr[0]), "+v"(hr[1]));
#pragma unroll
    for (int n = 0; n < 4; ++n) {
        const unsigned p0 = pv[n >> 1][2 * (n & 1)], p1 = pv[n >> 1][2 * (n & 1) + 1];
        *(LAS u32x2*)(lds + (16 * wm + l15) * OP + (64 * wn + 16 * n + 4 * g) * 2) =
            (u32x2){pack_bf16(acc[0][n][0] + bf_lo(p0), acc[0][n][1] + bf_hi(p0)), pack_bf16(acc[0][n][2] + bf_lo(p1), acc[0][n][3] + bf_hi(p1))};
    }
    wait_lgkm0();
    bar();
    STAMP(4, 4);
#pragma unroll
    for (int i = 0; i < 2; ++i) {
        const int r = 32 * i + (tid >> 4), trow = 32 * (r >> 4) + 16 * ks + (r & 15);
        const u32x4 av = *(LAS u32x4*)(lds + r * OP + (tid & 15) * 16);
        u32x4 o;
#pragma unroll
        for (int j = 0; j < 4; ++j) o[j] = pack_bf16(bf_lo(hr[i][j]) + bf_lo(av[j]), bf_hi(hr[i][j]) + bf_hi(av[j]));
        *(u32x4*)(out + (size_t)(pm * 128 + trow) * DM + pn * 128 + (tid & 15) * 8) = o;
    }
    STAMP(4, 7);
}

template <bool MEGA>
DEVI void phase3(const Ctx& c, const u16* __restrict__ wg, const u16* __restrict__ wu, const u16* __restrict__ wd) {
    using namespace g3;
    STAMP(3, 0);
    LAS char* lds = c.lds;
    const int tid = c.tid, lane = c.lane, w = c.w;
    const int grp = w >> 2, wm = w >> 1, wn = w & 1;
    const int l15 = lane & 15, g = lane >> 4, q4 = (lane >> 2) & 3, p4 = lane & 3;
    const G3Geom ge(c, wg, wu, wd);
    const int pm = ge.pm, pn = ge.pn;

    unsigned voffA[2];
#pragma unroll
    for (int j = 0; j < 2; ++j) {
        const int row = 8 * (2 * w + j) + (lane >> 3), ch = (lane & 7) ^ ((row >> 1) & 7);
        voffA[j] = (unsigned)(row * DM * 2 + ch * 16 - j * 1024);
    }
    const char* gA = c.outp + (size_t)pm * 256 * DM * 2;
    const unsigned ldsA = c.lds0 + 2 * w * 1024;

    if (w == 0) {
        const int cgp = (pn * 112 + (lane & 1) * 111) / 128 + ((lane >> 1) & 1) * 28, pb = ((lane >> 2) & 3) * 56 + cgp;
        const int ppm = (pb & 1) * 8 + ((pb >> 3) & 7), php = ((pb & 7) >> 1) * 4 + (pb >> 6);
        wait_flags(c.rs, lane < 32 ? FL_F4 + pm * 256 + lane * 8 : FL_F3 + ppm * 128 + php * 8, c.tag);
    }
    if (w == 0 || !MEGA) ge.prefetchB(c);
    bar();
    STAMP(3, 1);
    auto issueA = [&](int p, int slot) {
        const char* a = gA + (size_t)(p & 1) * AHALF + (p >> 1) * 128;
        glds<0>(a, voffA[0], ldsA + slot * A_PIECE); glds<1024>(a, voffA[1], ldsA + slot * A_PIECE);
    };
    issueA(0, 0); issueA(1, 1);
    f32x4 sv[4];
    asm_load4<SEQ * 8>(c.rs, WS_ST2 + ((tid & 3) * 4 * SEQ + pm * 256) * 8 + (tid >> 2) * 16, sv);
    float cp[4];
    {
        const int t2 = min(tid, 223);
        const unsigned o = WS_CS3 + (unsigned)(pn * 112 + t2 + (t2 >= 112 ? DF - 112 : 0)) * 4;
        asm volatile("buffer_load_dword %0, %4, %5, 0 offen sc1\n\tbuffer_load_dword %1, %4, %5, %6 offen sc1\n\t"
                     "buffer_load_dword %2, %4, %5, %7 offen sc1\n\tbuffer_load_dword %3, %4, %5, %8 offen sc1"
                     : "=&v"(cp[0]), "=&v"(cp[1]), "=&v"(cp[2]), "=&v"(cp[3])
                     : "v"(o), "s"(c.rs), "s"(7168 * 4), "s"(2 * 7168 * 4), "s"(3 * 7168 * 4)
                     : "memory");
    }
    issueA(2, 2);
    if (grp == 1) issueA(3, 3);
    LAS f32x4* rstat = (LAS f32x4*)(lds + TAB_OFF);
    LAS float* ctab = (LAS float*)(lds + TAB_OFF + 4096);
    auto row_table = [&]() {
        float mean[2], m2[2];
#pragma unroll
        for (int r = 0; r < 2; ++r) {
            mean[r] = 0.25f * ((sv[0][2 * r] + sv[1][2 * r]) + (sv[2][2 * r] + sv[3][2 * r]));
            float dd = 0.f, q = 0.f;
#pragma unroll
            for (int i = 0; i < 4; ++i) { const float d = sv[i][2 * r] - mean[r]; dd += d * d; q += sv[i][2 * r + 1]; }
            m2[r] = q + 64.f * dd;
        }
        const bool odd = tid & 1;
        const float mo = __shfl_xor(odd ? mean[0] : mean[1], 1), qo = __shfl_xor(odd ? m2[0] : m2[1], 1);
        const float mk = odd ? mean[1] : mean[0], qk = odd ? m2[1] : m2[0];
        const float d1 = mk - mo, ma = 0.5f * (mk + mo), qa = qk + qo + 128.f * d1 * d1;
        const float mb = __shfl_xor(ma, 2), qb = __shfl_xor(qa, 2);
        const float d = ma - mb;
        const float ve = (qa + qb + 256.f * d * d) * (1.f / DM) + LN_EPS;
        const float r = rsqrtf(ve);
        const float c1 = 2.3022081985f, c2 = 0.1029432397f;
        if ((tid & 2) == 0) rstat[2 * (tid >> 2) + (tid & 1)] = (f32x4){0.5f * (ma + mb), -c1 * r, -c2 * r * r * r, ve};
        asm volatile("" : "+v"(cp[0]), "+v"(cp[1]), "+v"(cp[2]), "+v"(cp[3]));
        if (tid < 224) ctab[tid] = (cp[0] + cp[1]) + (cp[2] + cp[3]);
    };

    int aoff[2];
#pragma unroll
    for (int ks = 0; ks < 2; ++ks) aoff[ks] = (64 * (wm & 1) + l15) * 128 + 16 * ((4 * ks + g) ^ (l15 >> 1));
    const int brow = (16 * (g >> 1) + 4 * (g & 1) + q4) * 224;
    const int boff = B_OFF + brow + 112 * wn + 8 * p4;
    const int boffX = B_OFF + brow + 112 * wn + 96 + (p4 < 2 ? 8 * p4 : B_SUB + 8 * (p4 - 2));

    f32x4 acc[4][7];
#pragma unroll
    for (int m = 0; m < 4; ++m)
#pragma unroll
        for (int n = 0; n < 7; ++n) acc[m][n] = (f32x4){0.f, 0.f, 0.f, 0.f};

    unsigned voffAe[2], voffAo[2], voffBo[2];
#pragma unroll
    for (int j = 0; j < 2; ++j) {
        voffAe[j] = voffA[j] + (grp ? 256u : AHALF + 128u);
        voffAo[j] = voffA[j] + (grp ? AHALF + 256u : 256u);
        voffBo[j] = ge.voffB[j] + BSTEP;
    }
    const char* pA = gA;
    const char* pB0 = ge.gB[0] + (size_t)4 * BSTEP;
    const char* pB1 = ge.gB[1] + (size_t)4 * BSTEP;
    int rdB = 0, prevB = 4 * B_PIECE;
    int rdA = grp * A_PIECE;
    int vB = boff, vBX = boffX, vA0 = aoff[0] + rdA, vA1 = aoff[1] + rdA;
    auto ringA = [&](int xx, int k) { xx += k * A_PIECE; return xx >= NR * A_PIECE ? xx - NR * A_PIECE : xx; };

    auto unit = [&](auto ks_tag, auto iss_tag, auto wv_tag) {
        constexpr int ks = decltype(ks_tag)::value, ISS = decltype(iss_tag)::value, WV = decltype(wv_tag)::value;
        bf16x8 a[4], b[7];
#pragma unroll
        for (int n = 0; n < 6; ++n) {
            LAS char* pb = lds + vB + (n >= 3 ? B_SUB : 0) + (n % 3) * 32;
            b[n] = lds_tr2(pb, pb + 8 * 224);
        }
        { LAS char* pb = lds + vBX; b[6] = lds_tr2(pb, pb + 8 * 224); }
#pragma unroll
        for (int m = 0; m < 4; ++m) a[m] = lds_b128(lds + (ks ? vA1 : vA0) + m * 2048);
        fence();
        const unsigned dA = ldsA + ringA(rdA, 3 + ks), dB = ge.ldsB + ((unsigned)prevB & ge.ringmask);
        const char* qA = pA; const char* qB0 = pB0; const char* qB1 = pB1;
        if constexpr (ISS == 1) {
            if constexpr (ks == 1) { pA += 128; pB0 += 2 * BSTEP; pB1 += 2 * BSTEP; }
        } else if constexpr (ISS == 2) {
            if (grp == 0) { glds<0>(qA, voffAe[0], dA); glds<1024>(qA, voffAe[1], dA); }
        }
        prevB = rdB;
        rdB = rdB == (NR - 1) * B_PIECE ? 0 : rdB + B_PIECE;
        vB = boff + rdB; vBX = boffX + rdB;
        if constexpr (ks == 1) { rdA = ringA(rdA, 2); vA0 = aoff[0] + rdA; vA1 = aoff[1] + rdA; }
        fence();
        if constexpr (WV >= 0) wait_vm<WV>();
        wait_lgkm0();
        fence();
        bar();
        fence();
        __builtin_amdgcn_s_setprio(1);
#pragma unroll
        for (int m = 0; m < 4; ++m) {
#pragma unroll
            for (int n = 0; n < 7; ++n) {
                acc[m][n] = mfma16(b[n], a[m], acc[m][n]);
                if constexpr (ISS == 1) {
                    if (n == 3) {
                        fence();
                        if (m == 0) glds<0>(qA, ks ? voffAo[0] : voffAe[0], dA);
                        if (m == 1) glds<1024>(qA, ks ? voffAo[1] : voffAe[1], dA);
                        if (m == 2) glds<0>(qB0, ks ? voffBo[0] : ge.voffB[0], dB);
                        if (m == 3) glds<1024>(qB1, ks ? voffBo[1] : ge.voffB[1], dB);
                        fence();
                    }
                }
                if constexpr (ISS == 3) {
                    if (n == 3 && m < 2) {
                        fence();
                        st128(c.rs, inbox_of(c.bid) + (2 * m + (tid >> 8)) * CHS + (tid & 255) * 16, (u32x4){SENT, SENT, SENT, SENT});
                        fence();
                    }
                }
            }
        }
        __builtin_amdgcn_s_setprio(0);
        fence();
        bar();
        fence();
    };
    wait_vm<12>();
    bar();
    if (grp == 1) bar();
    unit(IC(0), IC(1), IC(2));
    wait_regs4<6>(sv);
    row_table();
    unit(IC(1), IC(1), IC(4));
    for (int t = 1; t < 14; ++t) {
        unit(IC(0), IC(1), IC(6));
        unit(IC(1), IC(1), IC(6));
    }
    unit(IC(0), IC(2), IC(8));
    unit(IC(1), IC(0), IC(2));
    unit(IC(0), IC(3), IC(0));
    unit(IC(1), IC(0), IC(-1));
    if (grp == 0) bar();
    STAMP(3, 2);
    if constexpr (MEGA) {
        const RsGeom<true> gn(c, wd);
#pragma unroll
        for (int p = 0; p < 4; ++p) gn.pieceB(c, p);
    }
    f32x4 cw[7];
#pragma unroll
    for (int n = 0; n < 6; ++n) cw[n] = *(LAS f32x4*)(ctab + (n / 3) * 112 + wn * 56 + (n % 3) * 16 + 4 * g);
    cw[6] = *(LAS f32x4*)(ctab + (g >> 1) * 112 + wn * 56 + 48 + 4 * (g & 1));
    f32x4 st[4];
#pragma unroll
    for (int m = 0; m < 4; ++m) st[m] = rstat[64 * wm + 16 * m + l15];
    auto geglu2 = [](f32x2 G, f32x2 U, const f32x4& s) {
        const f32x2 a = G * (G * G * s[2] + s[1]);
        f32x2 e; e.x = __builtin_amdgcn_exp2f(a.x); e.y = __builtin_amdgcn_exp2f(a.y);
        const f32x2 d = e * s[3] + s[3];
        f32x2 r; r.x = __builtin_amdgcn_rcpf(d.x); r.y = __builtin_amdgcn_rcpf(d.y);
        const f32x2 o = (G * U) * r;
        return pack_bf16(o.x, o.y);
    };
    const unsigned gdst = WS_HH + (unsigned)((pn * 14 + wn * 7 + (g >> 1)) * CHS + (pm * 256 + 64 * wm + 16 * (g & 1) + l15) * 16);
    auto widen = [](u32x2 a, u32x2 b) {
        auto r0 = __builtin_amdgcn_permlane16_swap(a.x, b.x, false, false);
        auto r1 = __builtin_amdgcn_permlane16_swap(a.y, b.y, false, false);
        return (u32x4){r0[0], r1[0], r0[1], r1[1]};
    };
    u32x2 zz[2];
#pragma unroll
    for (int mp = 0; mp < 2; ++mp) {
#pragma unroll
        for (int n = 0; n < 3; ++n) {
            u32x2 o[2];
#pragma unroll
            for (int i = 0; i < 2; ++i) {
                const int m = 2 * mp + i;
                const f32x4 gg = acc[m][n] - st[m][0] * cw[n];
                const f32x4 uu = acc[m][n + 3] - st[m][0] * cw[n + 3];
                o[i].x = geglu2(gg.xy, uu.xy, st[m]);
                o[i].y = geglu2(gg.zw, uu.zw, st[m]);
            }
            st128(c.rs, gdst + n * (2 * CHS) + mp * 512, widen(o[0], o[1]));
            fence();
        }
        const f32x4 y0 = acc[2 * mp][6] - st[2 * mp][0] * cw[6];
        const f32x4 y1 = acc[2 * mp + 1][6] - st[2 * mp + 1][0] * cw[6];
        const f32x4 sx = lane < 32 ? st[2 * mp] : st[2 * mp + 1];
        f32x4 gg, uu;
#pragma unroll
        for (int j = 0; j < 4; ++j) {
            auto r = __builtin_amdgcn_permlane32_swap(__float_as_uint(y0[j]), __float_as_uint(y1[j]), false, false);
            gg[j] = __uint_as_float(r[0]); uu[j] = __uint_as_float(r[1]);
        }
        zz[mp] = (u32x2){geglu2(gg.xy, uu.xy, sx), geglu2(gg.zw, uu.zw, sx)};
        STAMP(3, 3 + mp);
    }
    st128(c.rs, WS_HH + (unsigned)((pn * 14 + wn * 7 + 6) * CHS + (pm * 256 + 64 * wm + 16 * (2 * (g & 1) + (g >> 1)) + l15) * 16), widen(zz[0], zz[1]));
    STAMP(3, 5);
    wait_vm<0>();
    bar();
    if (tid == 0) st64(c.rs, FL_F6 + pm * 256 + pn * 8, c.tag);
    STAMP(3, 7);
}

template <int PHASES>
__global__ __launch_bounds__(512, 2) void fused_kernel(const u16* __restrict__ x, const u16* __restrict__ wqkv,
                                                       const u16* __restrict__ wo, const u16* __restrict__ wg,
                                                       const u16* __restrict__ wu, const u16* __restrict__ wd,
                                                       u16* __restrict__ out, char* __restrict__ ws) {
    constexpr bool MEGA = PHASES == 15;
    extern __shared__ __attribute__((aligned(16))) char smem_raw[];
    Ctx c;
    c.lds = (LAS char*)smem_raw;
    c.lds0 = (unsigned)(uintptr_t)c.lds;
    c.rs = __builtin_amdgcn_make_buffer_rsrc((void*)ws, 0, WS_END, 0x00020000);
    c.ro = __builtin_amdgcn_make_buffer_rsrc((void*)out, 0, SEQ * DM * 2, 0x00020000);
    c.outp = (const char*)out;
    c.ws = ws;
    c.tid = threadIdx.x; c.lane = c.tid & 63;
    c.w = __builtin_amdgcn_readfirstlane(c.tid >> 6);
    c.bid = blockIdx.x;
    {
        asm volatile("buffer_load_dwordx2 %0, off, %1, 0 sc1" : "=v"(c.tag) : "s"(c.rs) : "memory");
        if constexpr (!(PHASES & 1)) finish_tag<0>(c);
    }
    if constexpr (PHASES & 1) phase1<MEGA>(c, x, wqkv, wo, wg, wu);
    if constexpr (PHASES & 2) phase2<MEGA>(c, wo, x, wg, wu, wd);
    if constexpr (PHASES & 4) phase3<MEGA>(c, wg, wu, wd);
    if constexpr (PHASES & 8) {
        phase4<MEGA>(c, wd, out);
        if (c.bid == 0 && c.tid == 0) st64(c.rs, FL_EPOCH, c.tag);
    }
}

template <int PHASES>
static void launch_phases(void* const* d_in, void* d_out, void* d_ws, hipStream_t stream) {
    (void)hipFuncSetAttribute((const void*)fused_kernel<PHASES>, hipFuncAttributeMaxDynamicSharedMemorySize, LDS_BYTES);
    fused_kernel<PHASES><<<256, 512, LDS_BYTES, stream>>>((const u16*)d_in[0], (const u16*)d_in[1], (const u16*)d_in[2],
                                                          (const u16*)d_in[4], (const u16*)d_in[5], (const u16*)d_in[6],
                                                          (u16*)d_out, (char*)d_ws);
}

extern "C" void kernel_launch(void* const* d_in, const int* in_sizes, int n_in, void* d_out, int out_size,
                              void* d_ws, size_t ws_size, hipStream_t stream) {
    launch_phases<15>(d_in, d_out, d_ws, stream);
}
```

```cpp
#include <hip/hip_runtime.h>
#include <hip/hip_bf16.h>
#include <stdint.h>
#include <type_traits>

#define LAS __attribute__((address_space(3)))
#define DEVI __device__ __forceinline__
#define IC(n) std::integral_constant<int, (n)>{}

typedef __attribute__((ext_vector_type(8))) __bf16 bf16x8;
typedef __attribute__((ext_vector_type(2))) __bf16 bf16x2;
typedef __attribute__((ext_vector_type(4))) short s16x4;
typedef __attribute__((ext_vector_type(8))) short s16x8;
typedef __attribute__((ext_vector_type(4))) float f32x4;
typedef __attribute__((ext_vector_type(2))) float f32x2;
typedef __attribute__((ext_vector_type(4))) unsigned u32x4;
typedef __attribute__((ext_vector_type(2))) unsigned u32x2;
typedef unsigned short u16;
typedef unsigned long long u64;
typedef __amdgpu_buffer_rsrc_t rsrc_t;

constexpr int SEQ = 2048, DM = 1024, NH = 32, DF = 3584;
constexpr float LN_EPS = 1e-5f;
constexpr int LDS_BYTES = 163840;
constexpr int SC1 = 16;

constexpr unsigned FL_EPOCH = 0;
constexpr unsigned FL_F1R = 1024;
constexpr unsigned FL_F1C = FL_F1R + 2048;
constexpr unsigned FL_F2 = FL_F1C + 2048;
constexpr unsigned FL_F3 = FL_F2 + 2048;
constexpr unsigned FL_F4 = FL_F3 + 2048;
constexpr unsigned FL_F6 = FL_F4 + 2048;
constexpr unsigned FL_F7 = FL_F6 + 2048;
#define CHPAD 256
constexpr int CHS = SEQ * 16 + CHPAD;
constexpr unsigned WS_O = 65536;
constexpr unsigned WS_H = WS_O + 128 * CHS;
constexpr unsigned WS_HH = WS_H + 128 * CHS;
constexpr unsigned WS_KVP = WS_HH + 448 * CHS;
constexpr unsigned WS_RS1 = WS_KVP + 256 * 2 * 1056 * 4;
constexpr unsigned WS_CS1 = WS_RS1 + 16 * 16 * 128 * 8;
constexpr unsigned WS_ST2 = WS_CS1 + 16 * 16 * 192 * 4;
constexpr unsigned WS_CS3 = WS_ST2 + SEQ * 16 * 8;
constexpr unsigned WS_END = WS_CS3 + 4 * 7168 * 4;
constexpr unsigned SENT = 0xffffffffu;

DEVI unsigned pack_bf16(float lo, float hi) {
    bf16x2 v; v.x = (__bf16)lo; v.y = (__bf16)hi;
    return __builtin_bit_cast(unsigned, v);
}
DEVI float bf_lo(unsigned u) { return __uint_as_float(u << 16); }
DEVI float bf_hi(unsigned u) { return __uint_as_float(u & 0xffff0000u); }
DEVI float round_bf16(float x) { return (float)(__bf16)x; }

template <int OFF, int POL = 0> DEVI void glds(const char* sbase, unsigned voff, unsigned lds_dst) {
    if constexpr (POL == 0)
        asm volatile("s_mov_b32 m0, %2\n\ts_nop 0\n\tglobal_load_lds_dwordx4 %0, %1 offset:%3 sc1"
                     : : "v"(voff), "s"(sbase), "s"(lds_dst), "n"(OFF) : "memory");
    else if constexpr (POL == 1)
        asm volatile("s_mov_b32 m0, %2\n\ts_nop 0\n\tglobal_load_lds_dwordx4 %0, %1 offset:%3 nt"
                     : : "v"(voff), "s"(sbase), "s"(lds_dst), "n"(OFF) : "memory");
    else
        asm volatile("s_mov_b32 m0, %2\n\ts_nop 0\n\tglobal_load_lds_dwordx4 %0, %1 offset:%3"
                     : : "v"(voff), "s"(sbase), "s"(lds_dst), "n"(OFF) : "memory");
}
#define POL1 2
#define POL0 2
#define POL2 2
DEVI bf16x8 lds_b128(LAS char* p) { return *(LAS bf16x8*)p; }
DEVI bf16x8 lds_tr2(LAS char* p0, LAS char* p1) {
    s16x4 a = __builtin_amdgcn_ds_read_tr16_b64_v4i16((LAS s16x4*)p0);
    s16x4 b = __builtin_amdgcn_ds_read_tr16_b64_v4i16((LAS s16x4*)p1);
    s16x8 r = __builtin_shufflevector(a, b, 0, 1, 2, 3, 4, 5, 6, 7);
    return __builtin_bit_cast(bf16x8, r);
}
DEVI f32x4 mfma16(bf16x8 a, bf16x8 b, f32x4 c) { return __builtin_amdgcn_mfma_f32_16x16x32_bf16(a, b, c, 0, 0, 0); }
template <int N> DEVI void wait_vm() { asm volatile("s_waitcnt vmcnt(%0)" ::"n"(N) : "memory"); }
DEVI void wait_lgkm0() { asm volatile("s_waitcnt lgkmcnt(0)" ::: "memory"); }
DEVI void bar() {
    asm volatile("" ::: "memory");
    __builtin_amdgcn_s_barrier();
    asm volatile("" ::: "memory");
}
DEVI void fence() { __builtin_amdgcn_sched_barrier(0); }
DEVI float xor16_32_sum(float v) { v += __shfl_xor(v, 16); v += __shfl_xor(v, 32); return v; }
DEVI float phi_f(float t) { return (t > 0.f ? t + 1.f : __expf(t)) + 1e-6f; }
DEVI bf16x8 ones8() {
    bf16x8 o;
#pragma unroll
    for (int j = 0; j < 8; ++j) o[j] = (__bf16)1.0f;
    return o;
}
DEVI void combine8(const f32x4 (&v)[4], float& mean, float& m2) {
    const float ms = ((v[0][0] + v[0][2]) + (v[1][0] + v[1][2])) + ((v[2][0] + v[2][2]) + (v[3][0] + v[3][2]));
    mean = ms * 0.125f;
    float dd = 0.f, q = 0.f;
#pragma unroll
    for (int i = 0; i < 4; ++i) {
        const float d0 = v[i][0] - mean, d1 = v[i][2] - mean;
        dd += d0 * d0 + d1 * d1;
        q += v[i][1] + v[i][3];
    }
    m2 = q + 64.f * dd;
}
DEVI u32x4 ld128(rsrc_t rs, unsigned off) { return __builtin_amdgcn_raw_buffer_load_b128(rs, off, 0, SC1); }
DEVI void st128(rsrc_t rs, unsigned off, u32x4 v) { __builtin_amdgcn_raw_buffer_store_b128(v, rs, off, 0, SC1); }
DEVI void st64(rsrc_t rs, unsigned off, u32x2 v) { __builtin_amdgcn_raw_buffer_store_b64(v, rs, off, 0, SC1); }
DEVI void st32(rsrc_t rs, unsigned off, unsigned v) { __builtin_amdgcn_raw_buffer_store_b32(v, rs, off, 0, SC1); }
DEVI u32x2 ld64(rsrc_t rs, unsigned off) { return __builtin_amdgcn_raw_buffer_load_b64(rs, off, 0, SC1); }
DEVI unsigned ld32(rsrc_t rs, unsigned off) { return __builtin_amdgcn_raw_buffer_load_b32(rs, off, 0, SC1); }
DEVI u32x4 no_sent(f32x4 v) {
    u32x4 u = __builtin_bit_cast(u32x4, v);
#pragma unroll
    for (int j = 0; j < 4; ++j) u[j] = min(u[j], SENT - 1u);
    return u;
}
DEVI unsigned umax4(u32x4 v) { return max(max(v[0], v[1]), max(v[2], v[3])); }
DEVI u32x4 widen(u32x2 a, u32x2 b) {
    auto r0 = __builtin_amdgcn_permlane16_swap(a.x, b.x, false, false);
    auto r1 = __builtin_amdgcn_permlane16_swap(a.y, b.y, false, false);
    return (u32x4){r0[0], r1[0], r0[1], r1[1]};
}
DEVI u32x2 pack4(f32x4 v) { return (u32x2){pack_bf16(v[0], v[1]), pack_bf16(v[2], v[3])}; }
DEVI bool timed_out(u64 t0) { return __builtin_amdgcn_s_memrealtime() - t0 > 1000000ull; }

DEVI void wait_flags(rsrc_t rs, unsigned off, u32x2 tag) {
    const u64 t0 = __builtin_amdgcn_s_memrealtime();
    for (;;) {
        const u32x2 v = ld64(rs, off);
        if (__all(v.x == tag.x && v.y == tag.y)) break;
        if (__builtin_amdgcn_s_memrealtime() - t0 > 1000000ull) break;
        __builtin_amdgcn_s_sleep(1);
    }
    asm volatile("" ::: "memory");
}

#define STAMP(kid, i)

struct Ctx {
    LAS char* lds;
    unsigned lds0;
    rsrc_t rs;
    const char* ws;
    u32x2 tag;
    int tid, lane, w, bid;
};
template <int N> DEVI void finish_tag(Ctx& c) {
    asm volatile("s_waitcnt vmcnt(%1)" : "+v"(c.tag) : "n"(N) : "memory");
    const unsigned ex = __builtin_amdgcn_readfirstlane(c.tag.x), ey = __builtin_amdgcn_readfirstlane(c.tag.y);
    c.tag.x = ex + 1u;
    c.tag.y = ey ^ 0x9e3779b9u;
}
template <int STRIDE> DEVI void asm_load4(rsrc_t rs, unsigned voff, f32x4 (&v)[4]) {
    asm volatile("buffer_load_dwordx4 %0, %4, %5, 0 offen sc1\n\tbuffer_load_dwordx4 %1, %4, %5, %6 offen sc1\n\t"
                 "buffer_load_dwordx4 %2, %4, %5, %7 offen sc1\n\tbuffer_load_dwordx4 %3, %4, %5, %8 offen sc1"
                 : "=&v"(v[0]), "=&v"(v[1]), "=&v"(v[2]), "=&v"(v[3])
                 : "v"(voff), "s"(rs), "s"(STRIDE), "s"(2 * STRIDE), "s"(3 * STRIDE)
                 : "memory");
}
template <int N> DEVI void wait_regs4(f32x4 (&v)[4]) {
    asm volatile("s_waitcnt vmcnt(%4)" : "+v"(v[0]), "+v"(v[1]), "+v"(v[2]), "+v"(v[3]) : "n"(N) : "memory");
}

namespace g2 { constexpr int B_RING = 65536, B_PIECE = 8192; }
namespace g4 { constexpr int B_RING = 81920, B_PIECE = 16384; }
namespace g3 {
constexpr int A_PIECE = 16384, B_SUB = 32 * 224, B_PIECE = 2 * B_SUB;
constexpr int NR = 5;
constexpr int B_OFF = NR * A_PIECE;
constexpr int TAB_OFF = B_OFF + NR * B_PIECE;
constexpr int SCR_OFF = TAB_OFF + 4096 + 1024;
constexpr int OPITCH = 240;
constexpr unsigned BSTEP = 32 * DF * 2, AHALF = 128 * 16, ASTEP = 8 * CHS;
}

DEVI unsigned inbox_of(int b) { return WS_O + (unsigned)((b & 1) * 4 + ((b >> 3) & 3)) * 4096u + (unsigned)(((b & 7) >> 1) * 8 + (b >> 5)) * (4u * CHS); }

template <bool G4> struct RsGeom {
    int pm, pn, ks;
    unsigned voffB[2];
    const char* gB;
    DEVI RsGeom(const Ctx& c, const u16* W) {
        const int xcd = c.bid & 7, loc = c.bid >> 3;
        if constexpr (G4) {
            ks = xcd & 1; pm = (xcd >> 1) * 4 + (loc & 3); pn = loc >> 2;
#pragma unroll
            for (int j = 0; j < 2; ++j) {
                const int row = 4 * (2 * c.w + j) + (c.lane >> 4), ch = c.lane & 15;
                const int s = (row & 3) | (((row >> 3) & 1) << 2);
                voffB[j] = (unsigned)(row * 2048 + pn * 256 + ((ch ^ (s << 1)) * 16) - j * 1024);
            }
            gB = (const char*)W + (size_t)ks * 1792 * 2048;
        } else {
            ks = 0; pm = (xcd & 3) * 4 + (loc & 3); pn = (xcd >> 2) * 8 + (loc >> 2);
            const int row = 8 * c.w + (c.lane >> 3), cb = c.lane & 7;
            const int f = ((row >> 1) & 1) | (((row >> 3) & 1) << 1);
            voffB[0] = (unsigned)(row * 2048 + pn * 128 + ((cb ^ (f << 1)) * 16));
            voffB[1] = 0;
            gB = (const char*)W;
        }
    }
    DEVI void pieceB(const Ctx& c, int p) const {
        const char* b = gB + (size_t)p * (64 * 2048);
        if constexpr (G4) {
            const unsigned d = c.lds0 + g4::B_RING + (p & 3) * g4::B_PIECE + 2 * c.w * 1024;
            glds<0>(b, voffB[0], d); glds<1024>(b, voffB[1], d);
        } else {
            glds<0, POL2>(b, voffB[0], c.lds0 + g2::B_RING + (p % 12) * g2::B_PIECE + c.w * 1024);
        }
    }
};

struct G3Geom {
    int pm, pn;
    unsigned voffB[2];
    const char* gB[2];
    unsigned ldsB, ringmask;
    DEVI G3Geom(const Ctx& c, const u16* wg, const u16* wu, const u16* wd) {
        using namespace g3;
        const int xcd = c.bid & 7, loc = c.bid >> 3;
        pm = (xcd & 1) * 4 + (loc & 3); pn = (xcd >> 1) * 8 + (loc >> 2);
        if (c.w < 7) {
#pragma unroll
            for (int j = 0; j < 2; ++j) {
                const int b = 2 * c.w + j, sub = b >= 7 ? 1 : 0;
                const int i = (b - 7 * sub) * 64 + c.lane, rho = i / 14, cc = i % 14;
                const int r = (rho & ~12) | ((rho & 4) << 1) | ((rho & 8) >> 1);
                voffB[j] = (unsigned)(r * DF * 2 + (pn * 112 + cc * 8) * 2);
                gB[j] = (const char*)(sub ? wu : wg) - j * 1024;
            }
            ldsB = c.lds0 + B_OFF + 2 * c.w * 1024;
            ringmask = ~0u;
        } else {
            const int l0 = min(c.lane & 7, 6);
            voffB[0] = (unsigned)((c.bid * 7 + l0) * 128);
            voffB[1] = (unsigned)((c.bid * 7 + l0) * 128 + 64);
            gB[0] = (const char*)wd; gB[1] = (const char*)wd - 1024;
            ldsB = c.lds0 + SCR_OFF;
            ringmask = 0u;
        }
    }
    DEVI void prefetchB(const Ctx& c) const {
#pragma unroll
        for (int p = 0; p < 4; ++p) {
            const unsigned d = ldsB + ((unsigned)(p * g3::B_PIECE) & ringmask);
            glds<0>(gB[0] + (size_t)p * g3::BSTEP, voffB[0], d);
            glds<1024>(gB[1] + (size_t)p * g3::BSTEP, voffB[1], d);
        }
    }
};

namespace g1 {
constexpr int A_BYTES = 16384, B_PITCH = 384, STAGE = 40960;
constexpr int TAB = 40960, CRED = 43008, KVT = 47104;
#define TCHMODE 0
#define NT0 0
#define TCH0 3
}

template <bool MEGA>
DEVI void phase1(Ctx& c, const u16* __restrict__ x, const u16* __restrict__ wqkv, const u16* __restrict__ wo,
                 const u16* __restrict__ wg, const u16* __restrict__ wu) {
    using namespace g1;
    STAMP(0, 0);
    LAS char* lds = c.lds;
    const int tid = c.tid, lane = c.lane, w = c.w;
    const int wm = w >> 1, wn = w & 1;
    const int xcd = c.bid & 7, loc = c.bid >> 3;
    const int pm = (xcd & 1) * 8 + (loc & 7);
    const int hp = (xcd >> 1) * 4 + (loc >> 3);
    const int s0 = (pm + hp) & 15;
    const int l15 = lane & 15, g = lane >> 4, q4 = (lane >> 2) & 3, p4 = lane & 3;

    unsigned voff[6], dst[6];
#pragma unroll
    for (int k = 0; k < 6; ++k) {
        const int i = w + 7 * k < 40 ? w + 7 * k : w + 7 * k - 7;
        const int rowa = 8 * i + (lane >> 3), ch = (lane & 7) ^ ((rowa >> 1) & 7);
        const int ib = (i - 16) * 64 + lane;
        const int row = ib / 24, cc = ib % 24, blk = cc >> 3, cb = cc & 7;
        const int f = ((row >> 1) & 1) | (((row >> 3) & 1) << 1);
        const unsigned oa = (unsigned)(rowa * DM * 2 + ch * 16);
        const unsigned ob = (unsigned)(row * (3 * DM) * 2 + (blk * DM + hp * 64 + (cb ^ (f << 1)) * 8) * 2);
        voff[k] = k < 2 ? oa : (k > 2 ? ob : (i < 16 ? oa : ob));
        dst[k] = c.lds0 + i * 1024;
    }
    const char* gA = (const char*)x + (size_t)pm * 128 * DM * 2;
    const char* gB = (const char*)wqkv;
    auto issue = [&](int s, int k) {
        const int ck = (s0 + s) & 15;
        const char* pa = gA + ck * 128;
        const char* pb = gB + (size_t)ck * (64 * 3 * DM * 2);
        if (s < NT0) glds<0, 1>(k < 2 ? pa : (k > 2 ? pb : (w < 2 ? pa : pb)), voff[k], dst[k] + (unsigned)((s & 3) * STAGE));
        else glds<0, POL1>(k < 2 ? pa : (k > 2 ? pb : (w < 2 ? pa : pb)), voff[k], dst[k] + (unsigned)((s & 3) * STAGE));
    };
    if (w < 7) {
#pragma unroll
        for (int s = 0; s < 4; ++s)
#pragma unroll
            for (int k = 0; k < 6; ++k) issue(s, k);
    }
    const unsigned kvslot = WS_KVP + (hp * 16 + pm) * (2 * 1056 * 4);
    {
        const u32x4 mk = {SENT, SENT, SENT, SENT};
        st128(c.rs, kvslot + tid * 16, mk);
        st128(c.rs, kvslot + 8192 + (tid & 15) * 16, mk);
    }
    fence();

    const int fB = ((q4 >> 1) & 1) | ((g & 1) << 1);
    int va[4][2], vb[4][2];
#pragma unroll
    for (int s = 0; s < 4; ++s)
#pragma unroll
        for (int i = 0; i < 2; ++i) {
            va[s][i] = s * STAGE + (32 * wm + l15) * 128 + 16 * ((4 * i + g) ^ (l15 >> 1));
            vb[s][i] = s * STAGE + A_BYTES + (8 * g + q4) * B_PITCH + 32 * ((2 * wn + i) ^ fB) + 8 * p4;
        }
    f32x4 acc[2][6];
#pragma unroll
    for (int m = 0; m < 2; ++m)
#pragma unroll
        for (int n = 0; n < 6; ++n) acc[m][n] = (f32x4){0.f, 0.f, 0.f, 0.f};

    const unsigned floff = lane < 16 ? FL_F1R + pm * 128 + l15 * 8 : FL_F1C + hp * 128 + l15 * 8;
    const unsigned vlin = (unsigned)lane * 16u;
    auto issue_stats = [&](int k) {
        const int i = w + 7 * k;
        glds<0>(c.ws + (i < 16 ? WS_RS1 + pm * 16384 + i * 1024 : WS_CS1 + hp * 12288 + (i - 16) * 1024), vlin, dst[k]);
    };
    const int cid = c.bid < 224 ? c.bid : c.bid - 32, cg = cid % 56, rr = cid / 56;
    const char* gC = (const char*)(cg < 28 ? wg : wu) + (size_t)(rr * 256 + w * 32) * (DF * 2) + (cg < 28 ? cg : cg - 28) * 256;
    const unsigned voffC = (unsigned)(g * (DF * 2) + l15 * 16);
    u32x4 cv[8];
    unsigned touch = 0;
    const char* gT = (const char*)(c.bid < 128 ? wg : wu) + (size_t)(c.bid & 127) * 57344;
    const RsGeom<false> gn(c, wo);

    LAS f32x2* rowtab = (LAS f32x2*)(lds + TAB);
    LAS float* coltab = (LAS float*)(lds + TAB + 1024);
    LAS unsigned* verdict = (LAS unsigned*)(lds + TAB + 1792);
    auto tables = [&]() {
        if (tid < 256) {
            const int row = tid >> 1, hf = tid & 1;
            f32x4 v[4];
#pragma unroll
            for (int i = 0; i < 4; ++i) {
                const f32x2 p0 = *(LAS f32x2*)(lds + ((8 * hf + 2 * i) * 128 + row) * 8);
                const f32x2 p1 = *(LAS f32x2*)(lds + ((8 * hf + 2 * i + 1) * 128 + row) * 8);
                v[i] = (f32x4){p0.x, p0.y, p1.x, p1.y};
            }
            float mean, m2;
            combine8(v, mean, m2);
            const float mo = __shfl_xor(mean, 1), m2o = __shfl_xor(m2, 1);
            const float d = mean - mo;
            const float var = (m2 + m2o + 256.f * d * d) * (1.f / DM);
            if (hf == 0) rowtab[row] = (f32x2){0.5f * (mean + mo), rsqrtf(var + LN_EPS)};
        } else if (tid < 448) {
            const int col = tid - 256;
            float s = 0.f;
#pragma unroll
            for (int i = 0; i < 16; ++i) s += *(LAS float*)(lds + A_BYTES + (i * 192 + col) * 4);
            coltab[col] = s;
        }
    };

    bf16x8 fa[2][2], fb[2][6];
    auto ldA = [&](int s, int kk, int m) { return lds_b128(lds + va[s][kk] + m * 2048); };
    auto ldB = [&](int s, int kk, int n) {
        LAS char* pb = lds + vb[s][n & 1] + kk * 32 * B_PITCH + (n >> 1) * 128;
        return lds_tr2(pb, pb + 4 * B_PITCH);
    };
    auto mainloop = [&](auto ld_tag) {
        constexpr bool LOADER = decltype(ld_tag)::value;
        u32x2 fl;
        auto vm_slot = [&](auto t_tag, int k) {
            constexpr int T = decltype(t_tag)::value;
            if constexpr (LOADER) {
                if constexpr (T == 9) {
                    if (k == 0) asm volatile("buffer_load_dwordx2 %0, %1, %2, 0 offen sc1" : "=v"(fl) : "v"(floff), "s"(c.rs) : "memory");
                }
                if constexpr (T >= 0 && T < 12) issue(T + 4, k);
                if constexpr (T == 12) { if (k < 4) issue_stats(k); }
            } else {
                if (k == 0) {
                    if constexpr (T == 2) {
                        asm volatile("global_load_dword %0, %1, %2" : "+v"(touch) : "v"(8 * vlin), "s"((const char*)wo + c.bid * 8192) : "memory");
                        asm volatile("global_load_dword %0, %1, %2" : "+v"(touch) : "v"(0u), "s"(c.ws + c.bid * 128) : "memory");
                    }
                    if constexpr (T >= TCH0 && T < TCH0 + 7)
                        asm volatile("global_load_dword %0, %1, %2" : "+v"(touch) : "v"(8 * vlin), "s"(gT + (T - TCH0) * 8192) : "memory");
                }
            }
            if constexpr (T == 13 && MEGA) { if (k < 2) gn.pieceB(c, k); }
            if constexpr (T == 14 && MEGA) { if (k < 5) gn.pieceB(c, 2 + k); }
            if constexpr (T == 15 && MEGA) { gn.pieceB(c, 7 + 2 * k); if (k < 2) gn.pieceB(c, 8 + 2 * k); }
        };
        auto half = [&](auto t_tag, auto kk_tag) {
            constexpr int t = decltype(t_tag)::value, kk = decltype(kk_tag)::value;
            constexpr int nt = kk ? t + 1 : t, nk = kk ^ 1, ns = nt & 3;
#pragma unroll
            for (int i = 0; i < 12; ++i) {
                const int m = i / 6, n = i % 6;
                if (n < 2) acc[m][n] = mfma16(fb[kk][n], fa[kk][m], acc[m][n]);
                else acc[m][n] = mfma16(fa[kk][m], fb[kk][n], acc[m][n]);
                if constexpr (nt < 16) {
                    if (i == 0) { fa[nk][0] = ldA(ns, nk, 0); fa[nk][1] = ldA(ns, nk, 1); }
                    else if (i <= 6) fb[nk][i - 1] = ldB(ns, nk, i - 1);
                }
                fence();
                if (kk == 1 && (i & 3) == 3) vm_slot(IC(t), i >> 2);
                if (kk == 0 && (i & 3) == 3) vm_slot(IC(t - 1), 3 + (i >> 2));
                fence();
            }
        };
        auto step = [&](auto t_tag, auto wv_tag) {
            constexpr int t = decltype(t_tag)::value, WV = decltype(wv_tag)::value;
            half(t_tag, IC(0));
            if constexpr (LOADER) { if constexpr (WV >= 0) wait_vm<WV>(); }
            else if constexpr (t == 2) wait_vm<0>();
            wait_lgkm0();
            fence();
            bar();
            fence();
            if constexpr (LOADER && t == 3) {
                if (tid < 2) st64(c.rs, tid ? FL_F1C + hp * 128 + pm * 8 : FL_F1R + pm * 128 + hp * 8, c.tag);
                fence();
            }
            if constexpr (t == 13) {
                bool ok = true;
                if constexpr (LOADER) {
                    asm volatile("" : "+v"(fl));
                    ok = __all(fl.x == c.tag.x && fl.y == c.tag.y);
                }
                if (lane == 0) verdict[w] = ok ? 0u : 1u;
                fence();
            }
            if constexpr (LOADER && t == 14) { tables(); fence(); }
            half(t_tag, IC(1));
        };
        step(IC(0), IC(16)); step(IC(1), IC(16)); step(IC(2), IC(16)); step(IC(3), IC(12));
        step(IC(4), IC(12)); step(IC(5), IC(12)); step(IC(6), IC(12)); step(IC(7), IC(12));
        step(IC(8), IC(12)); step(IC(9), IC(12)); step(IC(10), IC(12)); step(IC(11), IC(12));
        step(IC(12), IC(12)); step(IC(13), IC(10)); step(IC(14), IC(MEGA ? 2 : 0)); step(IC(15), IC(-1));
    };

    if (w < 7) wait_vm<20>(); else wait_vm<2>();
    finish_tag<26>(c);
    bar();
    STAMP(0, 1);
    fa[0][0] = ldA(0, 0, 0); fa[0][1] = ldA(0, 0, 1);
#pragma unroll
    for (int n = 0; n < 6; ++n) fb[0][n] = ldB(0, 0, n);
    {
        const int row = 16 * w + l15;
        const u32x4 r0 = __builtin_bit_cast(u32x4, lds_b128(lds + row * 128 + 16 * (g ^ (l15 >> 1))));
        const u32x4 r1 = __builtin_bit_cast(u32x4, lds_b128(lds + row * 128 + 16 * ((4 + g) ^ (l15 >> 1))));
        float v[16];
#pragma unroll
        for (int j = 0; j < 4; ++j) { v[2 * j] = bf_lo(r0[j]); v[2 * j + 1] = bf_hi(r0[j]); v[8 + 2 * j] = bf_lo(r1[j]); v[9 + 2 * j] = bf_hi(r1[j]); }
        float s = 0.f;
#pragma unroll
        for (int j = 0; j < 16; ++j) s += v[j];
        const float mu = xor16_32_sum(s) * (1.f / 64.f);
        float m2 = 0.f;
#pragma unroll
        for (int j = 0; j < 16; ++j) { const float d = v[j] - mu; m2 += d * d; }
        m2 = xor16_32_sum(m2);
        const int part = wm == 3 ? 0 : wm;
        f32x4 cs[2];
#pragma unroll
        for (int i = 0; i < 2; ++i) {
            cs[i] = (f32x4){0.f, 0.f, 0.f, 0.f};
#pragma unroll
            for (int kh = 0; kh < 2; ++kh) {
                LAS char* pb = lds + vb[0][i] + kh * 32 * B_PITCH + part * 128;
                cs[i] = mfma16(ones8(), lds_tr2(pb, pb + 4 * B_PITCH), cs[i]);
            }
        }
        st64(c.rs, WS_RS1 + ((pm * 16 + s0) * 128 + row) * 8, (u32x2){__float_as_uint(mu), __float_as_uint(m2)});
        st32(c.rs, WS_CS1 + ((hp * 16 + s0) * 192 + part * 64 + wn * 32 + (g & 1) * 16 + l15) * 4, __float_as_uint((g & 1) ? cs[1][0] : cs[0][0]));
    }
    fence();
    if (w < 7) mainloop(std::true_type{}); else mainloop(std::false_type{});
    STAMP(0, 2);
    {
        const u32x4 v0 = *(LAS u32x4*)verdict, v1 = *(LAS u32x4*)(verdict + 4);
        if (__builtin_amdgcn_readfirstlane(umax4(v0) | umax4(v1))) {
            wait_flags(c.rs, floff, c.tag);
            if (w < 7) {
#pragma unroll
                for (int j = 0; j < 4; ++j) issue_stats(j);
            }
            wait_vm<0>();
            bar();
            tables();
            wait_lgkm0();
            bar();
        }
    }
    const int rbase = 32 * wm;
    bf16x8 pk[2], pv[2];
    {
        f32x2 rt[2][4];
#pragma unroll
        for (int mm = 0; mm < 2; ++mm)
#pragma unroll
            for (int j = 0; j < 4; ++j) rt[mm][j] = rowtab[rbase + 16 * mm + 4 * g + j];
#pragma unroll
        for (int i = 0; i < 2; ++i) {
            const float cwk = coltab[64 + wn * 32 + i * 16 + l15], cwv = coltab[128 + wn * 32 + i * 16 + l15];
#pragma unroll
            for (int mm = 0; mm < 2; ++mm)
#pragma unroll
                for (int j = 0; j < 4; ++j) {
                    const float kk = rt[mm][j].y * (acc[mm][2 + i][j] - rt[mm][j].x * cwk);
                    const float vv = rt[mm][j].y * (acc[mm][4 + i][j] - rt[mm][j].x * cwv);
                    pk[i][mm * 4 + j] = (__bf16)phi_f(kk);
                    pv[i][mm * 4 + j] = (__bf16)vv;
                }
        }
    }
    LAS float* red = (LAS float*)lds;
    {
        LAS float* mine = red + w * 1056;
#pragma unroll
        for (int i = 0; i < 2; ++i) {
#pragma unroll
            for (int i2 = 0; i2 < 2; ++i2) {
                const f32x4 d = mfma16(pk[i], pv[i2], (f32x4){0.f, 0.f, 0.f, 0.f});
                *(LAS f32x4*)(mine + (16 * i2 + l15) * 32 + 16 * i + 4 * g) = d;
            }
            const f32x4 z = mfma16(pk[i], ones8(), (f32x4){0.f, 0.f, 0.f, 0.f});
            if (l15 == 0) *(LAS f32x4*)(mine + 1024 + 16 * i + 4 * g) = z;
        }
    }
    wait_lgkm0();
    bar();
    STAMP(0, 3);
    const int hh = tid >> 8, e4 = tid & 255;
    {
        f32x4 s = *(LAS f32x4*)(red + (hh + 0) * 1056 + e4 * 4);
        s += *(LAS f32x4*)(red + (hh + 2) * 1056 + e4 * 4);
        s += *(LAS f32x4*)(red + (hh + 4) * 1056 + e4 * 4);
        s += *(LAS f32x4*)(red + (hh + 6) * 1056 + e4 * 4);
        st128(c.rs, kvslot + tid * 16, no_sent(s));
        if (tid < 16) {
            const int h2 = tid >> 3, z4 = 1024 + (tid & 7) * 4;
            f32x4 z = *(LAS f32x4*)(red + (h2 + 0) * 1056 + z4);
            z += *(LAS f32x4*)(red + (h2 + 2) * 1056 + z4);
            z += *(LAS f32x4*)(red + (h2 + 4) * 1056 + z4);
            z += *(LAS f32x4*)(red + (h2 + 6) * 1056 + z4);
            st128(c.rs, kvslot + 8192 + tid * 16, no_sent(z));
        }
    }
    fence();
#pragma unroll
    for (int j = 0; j < 8; ++j)
        asm volatile("global_load_dwordx4 %0, %1, %2" : "=v"(cv[j]) : "v"(voffC), "s"(gC + (size_t)j * (4 * DF * 2)) : "memory");
    fence();
    bf16x8 pq[2];
#pragma unroll
    for (int mm = 0; mm < 2; ++mm) {
        const f32x2 rt = rowtab[rbase + 16 * mm + l15];
#pragma unroll
        for (int i = 0; i < 2; ++i) {
            const f32x4 cw = *(LAS f32x4*)(coltab + wn * 32 + i * 16 + 4 * g);
#pragma unroll
            for (int j = 0; j < 4; ++j) pq[mm][4 * i + j] = (__bf16)phi_f(rt.y * (acc[mm][i][j] - rt.x * cw[j]));
        }
    }
    asm volatile("" : "+v"(pq[0]), "+v"(pq[1]));
    STAMP(0, 4);
    LAS u16* kvt = (LAS u16*)(lds + KVT);
    {
        const unsigned src = WS_KVP + hp * 16 * (2 * 1056 * 4);
        const u64 t0 = __builtin_amdgcn_s_memrealtime();
        for (;;) {
            const unsigned v = ld32(c.rs, src + l15 * (2 * 1056 * 4) + w * 1024);
            if (__all(v != SENT) || timed_out(t0)) break;
            __builtin_amdgcn_s_sleep(1);
        }
        STAMP(0, 5);
    {
        asm volatile("s_waitcnt vmcnt(0)"
                     : "+v"(cv[0]), "+v"(cv[1]), "+v"(cv[2]), "+v"(cv[3]), "+v"(cv[4]), "+v"(cv[5]), "+v"(cv[6]), "+v"(cv[7])
                     : : "memory");
        asm volatile("" : "+v"(touch));
        u32x4 sel;
#pragma unroll
        for (int j = 0; j < 4; ++j) sel[j] = (l15 >> 1) == j ? ((l15 & 1) ? 0x3f800000u : 0x00003f80u) : 0u;
        f32x4 d = {0.f, 0.f, 0.f, 0.f};
#pragma unroll
        for (int j = 0; j < 8; ++j) d = mfma16(__builtin_bit_cast(bf16x8, sel), __builtin_bit_cast(bf16x8, cv[j]), d);
        if (g < 2) *(LAS f32x4*)(lds + CRED + w * 512 + (8 * l15 + 4 * g) * 4) = d;
    }
        auto gather = [&](auto z_tag) {
            constexpr bool Z = decltype(z_tag)::value;
            f32x4 s, z;
            for (;;) {
                u32x4 v[16], vz[4];
#pragma unroll
                for (int p = 0; p < 16; ++p) v[p] = ld128(c.rs, src + p * (2 * 1056 * 4) + tid * 16);
                if constexpr (Z) {
#pragma unroll
                    for (int p = 0; p < 4; ++p) vz[p] = ld128(c.rs, src + (4 * g + p) * (2 * 1056 * 4) + 8192 + l15 * 16);
                }
                s = (f32x4){0.f, 0.f, 0.f, 0.f}; z = s;
#pragma unroll
                for (int p = 0; p < 16; ++p) s += __builtin_bit_cast(f32x4, v[p]);
                if constexpr (Z) {
#pragma unroll
                    for (int p = 0; p < 4; ++p) z += __builtin_bit_cast(f32x4, vz[p]);
                }
                const f32x4 t = s + z;
                if (!__any(t[0] != t[0] || t[1] != t[1] || t[2] != t[2] || t[3] != t[3])) break;
                unsigned mx = 0;
#pragma unroll
                for (int p = 0; p < 16; ++p) mx = max(mx, umax4(v[p]));
                if constexpr (Z) {
#pragma unroll
                    for (int p = 0; p < 4; ++p) mx = max(mx, umax4(vz[p]));
                }
                if (!__any(mx == SENT) || timed_out(t0)) break;
            }
            *(LAS u32x2*)(kvt + hh * 1056 + e4 * 4) = (u32x2){pack_bf16(s[0], s[1]), pack_bf16(s[2], s[3])};
            if constexpr (Z) {
#pragma unroll
                for (int j = 0; j < 4; ++j) z[j] = xor16_32_sum(z[j]);
                if (lane < 16) *(LAS u32x2*)(kvt + (lane >> 3) * 1056 + 1024 + (lane & 7) * 4) = (u32x2){pack_bf16(z[0], z[1]), pack_bf16(z[2], z[3])};
            }
        };
        if (w == 1) gather(std::true_type{}); else gather(std::false_type{});
        wait_lgkm0();
        bar();
        STAMP(0, 6);
        LAS u16* kh = kvt + wn * 1056;
        const int e0 = 8 * (l15 >> 2) + (l15 & 3);
        auto rd8 = [&](LAS u16* p) {
            const u32x2 lo = *(LAS u32x2*)(p + 4 * g), hi = *(LAS u32x2*)(p + 16 + 4 * g);
            return __builtin_bit_cast(bf16x8, (u32x4){lo.x, lo.y, hi.x, hi.y});
        };
        const bf16x8 a0 = rd8(kh + e0 * 32), a1 = rd8(kh + (e0 + 4) * 32), az = rd8(kh + 1024);
        const f32x4 zero = {0.f, 0.f, 0.f, 0.f};
#pragma unroll
        for (int mm = 0; mm < 2; ++mm) {
            const f32x4 n0 = mfma16(a0, pq[mm], zero), n1 = mfma16(a1, pq[mm], zero), dn = mfma16(az, pq[mm], zero);
            const float inv = 1.f / (dn[0] + 1e-6f);
            u32x4 r;
            r.x = pack_bf16(n0[0] * inv, n0[1] * inv); r.y = pack_bf16(n0[2] * inv, n0[3] * inv);
            r.z = pack_bf16(n1[0] * inv, n1[1] * inv); r.w = pack_bf16(n1[2] * inv, n1[3] * inv);
            st128(c.rs, WS_O + (hp * 8 + wn * 4 + g) * CHS + (pm * 128 + rbase + 16 * mm + l15) * 16, r);
        }
        if (tid >= 480) {
            const int q = tid - 480;
            f32x4 s = {0.f, 0.f, 0.f, 0.f};
#pragma unroll
            for (int j = 0; j < 8; ++j) s += *(LAS f32x4*)(lds + CRED + j * 512 + q * 16);
            st128(c.rs, WS_CS3 + (rr * 7168 + cg * 128 + q * 4) * 4, __builtin_bit_cast(u32x4, s));
        }
    }
    wait_vm<0>();
    bar();
    if (tid == 0) st64(c.rs, FL_F3 + pm * 128 + hp * 8, c.tag);
    STAMP(0, 7);
}

template <bool MEGA>
DEVI void phase2(const Ctx& c, const u16* __restrict__ wo, const u16* __restrict__ x,
                 const u16* __restrict__ wg, const u16* __restrict__ wu, const u16* __restrict__ wd) {
    using namespace g2;
    STAMP(2, 0);
    LAS char* lds = c.lds;
    const int tid = c.tid, lane = c.lane, w = c.w;
    const int l15 = lane & 15, g = lane >> 4, q4 = (lane >> 2) & 3, p4 = lane & 3;
    const RsGeom<false> ge(c, wo);
    const int pm = ge.pm, pn = ge.pn;

    unsigned voffA[2];
#pragma unroll
    for (int j = 0; j < 2; ++j) {
        voffA[j] = (unsigned)((lane >> 3) * CHS + (8 * (2 * w + j) + (lane & 7)) * 16 + (1 - j) * 1024);
    }
    const char* gA = c.ws + WS_O + pm * 128 * 16 - 1024;
    const unsigned dA = c.lds0 + 2 * w * 1024;
    auto issueA = [&](int p, int j) {
        if (j == 0) glds<0>(gA + p * (8 * CHS), voffA[0], dA + (p & 3) * 16384);
        else glds<1024>(gA + p * (8 * CHS), voffA[1], dA + (p & 3) * 16384);
    };
    const int fB = ((q4 >> 1) & 1) | ((g & 1) << 1);
    const int va = (2 * w + (l15 >> 3)) * 1024 + g * 128 + (l15 & 7) * 16;
    int vb[2][4];
#pragma unroll
    for (int n = 0; n < 4; ++n) {
        vb[0][n] = B_RING + (8 * g + q4) * 128 + 32 * (n ^ fB) + 8 * p4;
        vb[1][n] = vb[0][n] + 6 * B_PIECE;
    }
    const int cq = 2 * (g & 1) + (g >> 1), row = pm * 128 + 16 * w + l15;
    const unsigned voffX = (unsigned)(row * DM * 2 + (pn * 8 + cq) * 16);
    if constexpr (!MEGA) {
#pragma unroll
        for (int p = 0; p < 12; ++p) ge.pieceB(c, p);
        wait_vm<0>();
    }
    fence();
    if (w == 0) wait_flags(c.rs, FL_F3 + pm * 128 + l15 * 8, c.tag);
    bar();
    STAMP(2, 1);
#pragma unroll
    for (int p = 0; p < 4; ++p) { issueA(p, 0); issueA(p, 1); }
    u32x4 xr[2];
    asm volatile("global_load_dwordx4 %0, %1, %2" : "=v"(xr[0]) : "v"(voffX), "s"(x) : "memory");
    asm volatile("global_load_dwordx4 %0, %1, %2 offset:64" : "=v"(xr[1]) : "v"(voffX), "s"(x) : "memory");

    f32x4 acc[4];
#pragma unroll
    for (int n = 0; n < 4; ++n) acc[n] = (f32x4){0.f, 0.f, 0.f, 0.f};
    bf16x8 fa[2], fb[2][4];
    auto ldA = [&](int p, int kk) { return lds_b128(lds + va + (p & 3) * 16384 + kk * 512); };
    auto ldB = [&](int p, int kk, int n) {
        const int s = p % 12;
        LAS char* pb = lds + vb[s / 6][n] + (s % 6) * B_PIECE + kk * 4096;
        return lds_tr2(pb, pb + 512);
    };
    auto half = [&](auto t_tag, auto kk_tag) {
        constexpr int t = decltype(t_tag)::value, kk = decltype(kk_tag)::value;
        constexpr int nt = kk ? t + 1 : t, nk = kk ^ 1;
#pragma unroll
        for (int i = 0; i < 4; ++i) {
            if constexpr (nt < 16) {
                if (i == 0) { fa[nk] = ldA(nt, nk); fb[nk][0] = ldB(nt, nk, 0); fb[nk][1] = ldB(nt, nk, 1); }
                if (i == 1) { fb[nk][2] = ldB(nt, nk, 2); fb[nk][3] = ldB(nt, nk, 3); }
            }
            acc[i] = mfma16(fb[kk][i], fa[kk], acc[i]);
            fence();
            if constexpr (kk == 1 && t + 4 < 16) {
                if (i == 1 || i == 2) issueA(t + 4, i - 1);
                if (i == 3 && t == 3) { ge.pieceB(c, 12); ge.pieceB(c, 13); ge.pieceB(c, 14); ge.pieceB(c, 15); }
            }
            fence();
        }
    };
    auto step = [&](auto t_tag, auto wv_tag) {
        constexpr int t = decltype(t_tag)::value, WV = decltype(wv_tag)::value;
        half(t_tag, IC(0));
        if constexpr (WV >= 0) wait_vm<WV>();
        wait_lgkm0();
        fence();
        if constexpr (t == 3 || t == 11 || t == 15) bar();
        fence();
        half(t_tag, IC(1));
    };
    wait_vm<8>();
    fa[0] = ldA(0, 0);
#pragma unroll
    for (int n = 0; n < 4; ++n) fb[0][n] = ldB(0, 0, n);
    fence();
    step(IC(0), IC(6)); step(IC(1), IC(6)); step(IC(2), IC(6)); step(IC(3), IC(4));
    step(IC(4), IC(8)); step(IC(5), IC(8)); step(IC(6), IC(8)); step(IC(7), IC(4));
    step(IC(8), IC(4)); step(IC(9), IC(4)); step(IC(10), IC(4)); step(IC(11), IC(4));
    step(IC(12), IC(4)); step(IC(13), IC(2)); step(IC(14), IC(0)); step(IC(15), IC(-1));
    STAMP(2, 2);
    asm volatile("" : "+v"(xr[0]), "+v"(xr[1]));

    float hv[16];
#pragma unroll
    for (int i = 0; i < 2; ++i) {
        const u32x4 av = widen(pack4(acc[2 * i]), pack4(acc[2 * i + 1]));
        u32x4 hb;
#pragma unroll
        for (int j = 0; j < 4; ++j) {
            hb[j] = pack_bf16(bf_lo(xr[i][j]) + bf_lo(av[j]), bf_hi(xr[i][j]) + bf_hi(av[j]));
            hv[8 * i + 2 * j] = bf_lo(hb[j]); hv[8 * i + 2 * j + 1] = bf_hi(hb[j]);
        }
        st128(c.rs, WS_H + (pn * 8 + 4 * i + cq) * CHS + row * 16, hb);
    }
    fence();
    {
        float s = 0.f;
#pragma unroll
        for (int j = 0; j < 16; ++j) s += hv[j];
        const float mu = xor16_32_sum(s) * (1.f / 64.f);
        float m2 = 0.f;
#pragma unroll
        for (int j = 0; j < 16; ++j) { const float d = hv[j] - mu; m2 += d * d; }
        m2 = xor16_32_sum(m2);
        if (lane < 16) st64(c.rs, WS_ST2 + (pn * SEQ + row) * 8, (u32x2){__float_as_uint(mu), __float_as_uint(m2)});
    }
    STAMP(2, 4);
    if (MEGA && w != 0) {
        const G3Geom gn(c, wg, wu, wd);
        gn.prefetchB(c);
        wait_vm<8>();
    } else {
        wait_vm<0>();
    }
    bar();
    if (tid == 0) st64(c.rs, FL_F4 + pm * 128 + pn * 8, c.tag);
    STAMP(2, 7);
}

template <bool MEGA>
DEVI void phase4(const Ctx& c, const u16* __restrict__ wd, u16* __restrict__ out) {
    using namespace g4;
    STAMP(4, 0);
    LAS char* lds = c.lds;
    const int tid = c.tid, lane = c.lane, w = c.w;
    const int wm = w >> 1, wn = w & 1;
    const int l15 = lane & 15, g = lane >> 4, q4 = (lane >> 2) & 3, p4 = lane & 3;
    const RsGeom<true> ge(c, wd);
    const int pm = ge.pm, pn = ge.pn, ks = ge.ks;

    unsigned voffA[2];
#pragma unroll
    for (int j = 0; j < 2; ++j) {
        voffA[j] = (unsigned)((lane >> 3) * CHS + (8 * (2 * w + j) + (lane & 7)) * 16 + (1 - j) * 1024);
    }
    constexpr int ASTEP = 8 * CHS;
    const char* gA = c.ws + WS_HH + (size_t)ks * 28 * ASTEP + pm * 128 * 16 - 1024;
    const unsigned dA = c.lds0 + 2 * w * 1024, dB = c.lds0 + B_RING + 2 * w * 1024;
    int va[2][2], vb[4];
#pragma unroll
    for (int kk = 0; kk < 2; ++kk)
#pragma unroll
        for (int m = 0; m < 2; ++m) va[kk][m] = (4 * wm + 2 * (m ^ ks) + (l15 >> 3)) * 1024 + (4 * kk + g) * 128 + (l15 & 7) * 16;
    {
        const int sB = q4 | ((g & 1) << 2);
#pragma unroll
        for (int n = 0; n < 4; ++n) vb[n] = B_RING + (8 * g + q4) * 256 + 32 * ((4 * wn + n) ^ sB) + 8 * p4;
    }
    if constexpr (!MEGA) {
#pragma unroll
        for (int p = 0; p < 4; ++p) ge.pieceB(c, p);
        wait_vm<0>();
    }
    fence();
    if (w == 0) {
        const int pb = c.bid ^ 1, ppm = (pb & 1) * 4 + ((pb >> 3) & 3), ppn = ((pb & 7) >> 1) * 8 + (pb >> 5);
        wait_flags(c.rs, lane < 16 ? FL_F6 + (pm >> 1) * 256 + ks * 128 + l15 * 8 : FL_F6 + ppm * 256 + ppn * 8, c.tag);
    }
    bar();
    STAMP(4, 1);
#pragma unroll
    for (int p = 0; p < 4; ++p) { glds<0>(gA + p * ASTEP, voffA[0], dA + p * 16384); glds<1024>(gA + p * ASTEP, voffA[1], dA + p * 16384); }

    f32x4 acc[2][4];
#pragma unroll
    for (int m = 0; m < 2; ++m)
#pragma unroll
        for (int n = 0; n < 4; ++n) acc[m][n] = (f32x4){0.f, 0.f, 0.f, 0.f};
    bf16x8 fa[2][2], fb[2][4];
    auto ldA = [&](int s, int kk, int m) { return lds_b128(lds + va[kk][m] + s * 16384); };
    auto ldB = [&](int s, int kk, int n) {
        LAS char* pb = lds + vb[n] + s * B_PIECE + kk * 8192;
        return lds_tr2(pb, pb + 1024);
    };
    u32x4 hr[2];
    const int cq = pn * 16 + wn * 8 + 2 * (g & 1) + (g >> 1), orow = pm * 128 + 32 * wm + 16 * ks + l15;
    const unsigned voffH = WS_H + (unsigned)(cq * CHS + orow * 16);
    const char* pa = gA + 4 * ASTEP;
    const char* pb = ge.gB + (size_t)4 * (64 * 2048);
    auto half = [&](auto s_tag, auto kk_tag, auto iss_tag, auto last_tag) {
        constexpr int s = decltype(s_tag)::value, kk = decltype(kk_tag)::value, ISS = decltype(iss_tag)::value;
        constexpr bool LAST = decltype(last_tag)::value;
        constexpr int ns = kk ? (s + 1) & 3 : s, nk = kk ^ 1;
#pragma unroll
        for (int i = 0; i < 8; ++i) {
            const int m = i >> 2, n = i & 3;
            acc[m][n] = mfma16(fb[kk][n], fa[kk][m], acc[m][n]);
            if constexpr (!LAST) {
                if (i == 0) { fa[nk][0] = ldA(ns, nk, 0); fa[nk][1] = ldA(ns, nk, 1); }
                else if (i <= 4) fb[nk][i - 1] = ldB(ns, nk, i - 1);
            }
            fence();
            if constexpr (kk == 1 && ISS == 1) {
                if (i == 1) glds<0>(pa + s * ASTEP, voffA[0], dA + s * 16384);
                if (i == 3) glds<1024>(pa + s * ASTEP, voffA[1], dA + s * 16384);
                if (i == 5) glds<0>(pb + (size_t)s * (64 * 2048), ge.voffB[0], dB + s * B_PIECE);
                if (i == 7) glds<1024>(pb + (size_t)s * (64 * 2048), ge.voffB[1], dB + s * B_PIECE);
            }
            if constexpr (kk == 1 && ISS == 2) {
                if (i == 1) asm volatile("buffer_load_dwordx4 %0, %1, %2, 0 offen sc1" : "=v"(hr[0]) : "v"(voffH), "s"(c.rs) : "memory");
                if (i == 3) asm volatile("buffer_load_dwordx4 %0, %1, %2, %3 offen sc1" : "=v"(hr[1]) : "v"(voffH), "s"(c.rs), "s"(4 * CHS) : "memory");
            }
            fence();
        }
    };
    auto step = [&](auto s_tag, auto iss_tag, auto wv_tag) {
        constexpr int WV = decltype(wv_tag)::value;
        half(s_tag, IC(0), iss_tag, std::false_type{});
        if constexpr (WV >= 0) {
            wait_vm<WV>();
            wait_lgkm0();
            fence();
            bar();
            fence();
        }
        half(s_tag, IC(1), iss_tag, std::integral_constant<bool, (WV < 0)>{});
    };
    wait_vm<6>();
    bar();
    fa[0][0] = ldA(0, 0, 0); fa[0][1] = ldA(0, 0, 1);
#pragma unroll
    for (int n = 0; n < 4; ++n) fb[0][n] = ldB(0, 0, n);
    fence();
    step(IC(0), IC(1), IC(4)); step(IC(1), IC(1), IC(6)); step(IC(2), IC(1), IC(8)); step(IC(3), IC(1), IC(8));
    for (int it = 0; it < 5; ++it) {
        pa += 4 * ASTEP; pb += (size_t)4 * (64 * 2048);
        step(IC(0), IC(1), IC(8)); step(IC(1), IC(1), IC(8)); step(IC(2), IC(1), IC(8)); step(IC(3), IC(1), IC(8));
    }
    step(IC(0), IC(0), IC(8)); step(IC(1), IC(0), IC(4)); step(IC(2), IC(2), IC(0)); step(IC(3), IC(0), IC(-1));
    STAMP(4, 2);

    const unsigned xs = inbox_of(c.bid ^ 1) + (w >> 1) * CHS + (w & 1) * 2048 + lane * 16;
#pragma unroll
    for (int n = 0; n < 2; ++n) {
        u32x4 v = {pack_bf16(acc[1][2 * n][0], acc[1][2 * n][1]), pack_bf16(acc[1][2 * n][2], acc[1][2 * n][3]),
                   pack_bf16(acc[1][2 * n + 1][0], acc[1][2 * n + 1][1]), pack_bf16(acc[1][2 * n + 1][2], acc[1][2 * n + 1][3])};
        st128(c.rs, xs + n * 1024, no_sent(__builtin_bit_cast(f32x4, v)));
    }
    const unsigned xr = inbox_of(c.bid) + (w >> 1) * CHS + (w & 1) * 2048 + lane * 16;
    u32x4 pv[2];
    {
        const u64 t0 = __builtin_amdgcn_s_memrealtime();
        for (;;) {
#pragma unroll
            for (int n = 0; n < 2; ++n) pv[n] = ld128(c.rs, xr + n * 1024);
            const unsigned mx = max(umax4(pv[0]), umax4(pv[1]));
            if (!__any(mx == SENT) || timed_out(t0)) break;
            __builtin_amdgcn_s_sleep(1);
        }
    }
    STAMP(4, 3);
    asm volatile("" : "+v"(hr[0]), "+v"(hr[1]));
#pragma unroll
    for (int i = 0; i < 2; ++i) {
        u32x2 d[2];
#pragma unroll
        for (int k = 0; k < 2; ++k) {
            const unsigned p0 = pv[i][2 * k], p1 = pv[i][2 * k + 1];
            const f32x4 a = acc[0][2 * i + k];
            d[k] = (u32x2){pack_bf16(a[0] + bf_lo(p0), a[1] + bf_hi(p0)), pack_bf16(a[2] + bf_lo(p1), a[3] + bf_hi(p1))};
        }
        const u32x4 av = widen(d[0], d[1]);
        u32x4 o;
#pragma unroll
        for (int j = 0; j < 4; ++j) o[j] = pack_bf16(bf_lo(hr[i][j]) + bf_lo(av[j]), bf_hi(hr[i][j]) + bf_hi(av[j]));
        *(u32x4*)(out + (size_t)orow * DM + (cq + 4 * i) * 8) = o;
    }
    STAMP(4, 7);
}

template <bool MEGA>
DEVI void phase3(const Ctx& c, const u16* __restrict__ wg, const u16* __restrict__ wu, const u16* __restrict__ wd) {
    using namespace g3;
    STAMP(3, 0);
    LAS char* lds = c.lds;
    const int tid = c.tid, lane = c.lane, w = c.w;
    const int grp = w >> 2, wm = w >> 1, wn = w & 1;
    const int l15 = lane & 15, g = lane >> 4, q4 = (lane >> 2) & 3, p4 = lane & 3;
    const G3Geom ge(c, wg, wu, wd);
    const int pm = ge.pm, pn = ge.pn;

    unsigned voffA[2];
#pragma unroll
    for (int j = 0; j < 2; ++j) {
        voffA[j] = (unsigned)((lane >> 3) * CHS + (8 * (2 * w + j) + (lane & 7)) * 16 + (1 - j) * 1024);
    }
    const char* gA = c.ws + WS_H + pm * 256 * 16 - 1024;
    const unsigned ldsA = c.lds0 + 2 * w * 1024;

    if (w == 0) {
        const int cgp = (pn * 112 + (lane & 1) * 111) / 128 + ((lane >> 1) & 1) * 28, pb = ((lane >> 2) & 3) * 56 + cgp;
        const int ppm = (pb & 1) * 8 + ((pb >> 3) & 7), php = ((pb & 7) >> 1) * 4 + (pb >> 6);
        wait_flags(c.rs, lane < 32 ? FL_F4 + pm * 256 + lane * 8 : FL_F3 + ppm * 128 + php * 8, c.tag);
    }
    if (w == 0 || !MEGA) ge.prefetchB(c);
    bar();
    STAMP(3, 1);
    auto issueA = [&](int p, int slot) {
        const char* a = gA + (size_t)(p & 1) * AHALF + (p >> 1) * ASTEP;
        glds<0>(a, voffA[0], ldsA + slot * A_PIECE); glds<1024>(a, voffA[1], ldsA + slot * A_PIECE);
    };
    issueA(0, 0); issueA(1, 1);
    f32x4 sv[4];
    asm_load4<SEQ * 8>(c.rs, WS_ST2 + ((tid & 3) * 4 * SEQ + pm * 256) * 8 + (tid >> 2) * 16, sv);
    float cp[4];
    {
        const int t2 = min(tid, 223);
        const unsigned o = WS_CS3 + (unsigned)(pn * 112 + t2 + (t2 >= 112 ? DF - 112 : 0)) * 4;
        asm volatile("buffer_load_dword %0, %4, %5, 0 offen sc1\n\tbuffer_load_dword %1, %4, %5, %6 offen sc1\n\t"
                     "buffer_load_dword %2, %4, %5, %7 offen sc1\n\tbuffer_load_dword %3, %4, %5, %8 offen sc1"
                     : "=&v"(cp[0]), "=&v"(cp[1]), "=&v"(cp[2]), "=&v"(cp[3])
                     : "v"(o), "s"(c.rs), "s"(7168 * 4), "s"(2 * 7168 * 4), "s"(3 * 7168 * 4)
                     : "memory");
    }
    issueA(2, 2);
    if (grp == 1) issueA(3, 3);
    LAS f32x4* rstat = (LAS f32x4*)(lds + TAB_OFF);
    LAS float* ctab = (LAS float*)(lds + TAB_OFF + 4096);
    auto row_table = [&]() {
        float mean[2], m2[2];
#pragma unroll
        for (int r = 0; r < 2; ++r) {
            mean[r] = 0.25f * ((sv[0][2 * r] + sv[1][2 * r]) + (sv[2][2 * r] + sv[3][2 * r]));
            float dd = 0.f, q = 0.f;
#pragma unroll
            for (int i = 0; i < 4; ++i) { const float d = sv[i][2 * r] - mean[r]; dd += d * d; q += sv[i][2 * r + 1]; }
            m2[r] = q + 64.f * dd;
        }
        const bool odd = tid & 1;
        const float mo = __shfl_xor(odd ? mean[0] : mean[1], 1), qo = __shfl_xor(odd ? m2[0] : m2[1], 1);
        const float mk = odd ? mean[1] : mean[0], qk = odd ? m2[1] : m2[0];
        const float d1 = mk - mo, ma = 0.5f * (mk + mo), qa = qk + qo + 128.f * d1 * d1;
        const float mb = __shfl_xor(ma, 2), qb = __shfl_xor(qa, 2);
        const float d = ma - mb;
        const float ve = (qa + qb + 256.f * d * d) * (1.f / DM) + LN_EPS;
        const float r = rsqrtf(ve);
        const float c1 = 2.3022081985f, c2 = 0.1029432397f;
        if ((tid & 2) == 0) rstat[2 * (tid >> 2) + (tid & 1)] = (f32x4){0.5f * (ma + mb), -c1 * r, -c2 * r * r * r, ve};
        asm volatile("" : "+v"(cp[0]), "+v"(cp[1]), "+v"(cp[2]), "+v"(cp[3]));
        if (tid < 224) ctab[tid] = (cp[0] + cp[1]) + (cp[2] + cp[3]);
    };

    int aoff[2];
#pragma unroll
    for (int ks = 0; ks < 2; ++ks) aoff[ks] = (8 * (wm & 1) + (l15 >> 3)) * 1024 + (4 * ks + g) * 128 + (l15 & 7) * 16;
    const int brow = (16 * (g >> 1) + 4 * (g & 1) + q4) * 224;
    const int boff = B_OFF + brow + 112 * wn + 8 * p4;
    const int boffX = B_OFF + brow + 112 * wn + 96 + (p4 < 2 ? 8 * p4 : B_SUB + 8 * (p4 - 2));

    f32x4 acc[4][7];
#pragma unroll
    for (int m = 0; m < 4; ++m)
#pragma unroll
        for (int n = 0; n < 7; ++n) acc[m][n] = (f32x4){0.f, 0.f, 0.f, 0.f};

    unsigned voffAe[2], voffAo[2], voffBo[2];
#pragma unroll
    for (int j = 0; j < 2; ++j) {
        voffAe[j] = voffA[j] + (grp ? 2 * ASTEP : AHALF + ASTEP);
        voffAo[j] = voffA[j] + (grp ? AHALF + 2 * ASTEP : 2 * ASTEP);
        voffBo[j] = ge.voffB[j] + BSTEP;
    }
    const char* pA = gA;
    const char* pB0 = ge.gB[0] + (size_t)4 * BSTEP;
    const char* pB1 = ge.gB[1] + (size_t)4 * BSTEP;
    int rdB = 0, prevB = 4 * B_PIECE;
    int rdA = grp * A_PIECE;
    int vB = boff, vBX = boffX, vA0 = aoff[0] + rdA, vA1 = aoff[1] + rdA;
    auto ringA = [&](int xx, int k) { xx += k * A_PIECE; return xx >= NR * A_PIECE ? xx - NR * A_PIECE : xx; };

    auto unit = [&](auto ks_tag, auto iss_tag, auto wv_tag) {
        constexpr int ks = decltype(ks_tag)::value, ISS = decltype(iss_tag)::value, WV = decltype(wv_tag)::value;
        bf16x8 a[4], b[7];
#pragma unroll
        for (int n = 0; n < 6; ++n) {
            LAS char* pb = lds + vB + (n >= 3 ? B_SUB : 0) + (n % 3) * 32;
            b[n] = lds_tr2(pb, pb + 8 * 224);
        }
        { LAS char* pb = lds + vBX; b[6] = lds_tr2(pb, pb + 8 * 224); }
#pragma unroll
        for (int m = 0; m < 4; ++m) a[m] = lds_b128(lds + (ks ? vA1 : vA0) + m * 2048);
        fence();
        const unsigned dA = ldsA + ringA(rdA, 3 + ks), dB = ge.ldsB + ((unsigned)prevB & ge.ringmask);
        const char* qA = pA; const char* qB0 = pB0; const char* qB1 = pB1;
        if constexpr (ISS == 1) {
            if constexpr (ks == 1) { pA += ASTEP; pB0 += 2 * BSTEP; pB1 += 2 * BSTEP; }
        } else if constexpr (ISS == 2) {
            if (grp == 0) { glds<0>(qA, voffAe[0], dA); glds<1024>(qA, voffAe[1], dA); }
        }
        prevB = rdB;
        rdB = rdB == (NR - 1) * B_PIECE ? 0 : rdB + B_PIECE;
        vB = boff + rdB; vBX = boffX + rdB;
        if constexpr (ks == 1) { rdA = ringA(rdA, 2); vA0 = aoff[0] + rdA; vA1 = aoff[1] + rdA; }
        fence();
        if constexpr (WV >= 0) wait_vm<WV>();
        wait_lgkm0();
        fence();
        bar();
        fence();
        __builtin_amdgcn_s_setprio(1);
#pragma unroll
        for (int m = 0; m < 4; ++m) {
#pragma unroll
            for (int n = 0; n < 7; ++n) {
                acc[m][n] = mfma16(b[n], a[m], acc[m][n]);
                if constexpr (ISS == 1) {
                    if (n == 3) {
                        fence();
                        if (m == 0) glds<0>(qA, ks ? voffAo[0] : voffAe[0], dA);
                        if (m == 1) glds<1024>(qA, ks ? voffAo[1] : voffAe[1], dA);
                        if (m == 2) glds<0>(qB0, ks ? voffBo[0] : ge.voffB[0], dB);
                        if (m == 3) glds<1024>(qB1, ks ? voffBo[1] : ge.voffB[1], dB);
                        fence();
                    }
                }
                if constexpr (ISS == 3) {
                    if (n == 3 && m < 2) {
                        fence();
                        st128(c.rs, inbox_of(c.bid) + (2 * m + (tid >> 8)) * CHS + (tid & 255) * 16, (u32x4){SENT, SENT, SENT, SENT});
                        fence();
                    }
                }
            }
        }
        __builtin_amdgcn_s_setprio(0);
        fence();
        if (WV >= 0 || grp == 0) bar();
        fence();
    };
    wait_vm<12>();
    bar();
    if (grp == 1) bar();
    unit(IC(0), IC(1), IC(2));
    wait_regs4<6>(sv);
    row_table();
    unit(IC(1), IC(1), IC(4));
    for (int t = 1; t < 14; ++t) {
        unit(IC(0), IC(1), IC(6));
        unit(IC(1), IC(1), IC(6));
    }
    unit(IC(0), IC(2), IC(8));
    unit(IC(1), IC(0), IC(2));
    unit(IC(0), IC(3), IC(0));
    unit(IC(1), IC(0), IC(-1));
    STAMP(3, 2);
    if constexpr (MEGA) {
        const RsGeom<true> gn(c, wd);
#pragma unroll
        for (int p = 0; p < 4; ++p) gn.pieceB(c, p);
    }
    f32x4 cw[7];
#pragma unroll
    for (int n = 0; n < 6; ++n) cw[n] = *(LAS f32x4*)(ctab + (n / 3) * 112 + wn * 56 + (n % 3) * 16 + 4 * g);
    cw[6] = *(LAS f32x4*)(ctab + (g >> 1) * 112 + wn * 56 + 48 + 4 * (g & 1));
    f32x4 st[4];
#pragma unroll
    for (int m = 0; m < 4; ++m) st[m] = rstat[64 * wm + 16 * m + l15];
    auto geglu2 = [](f32x2 G, f32x2 U, const f32x4& s) {
        const f32x2 a = G * (G * G * s[2] + s[1]);
        f32x2 e; e.x = __builtin_amdgcn_exp2f(a.x); e.y = __builtin_amdgcn_exp2f(a.y);
        const f32x2 d = e * s[3] + s[3];
        f32x2 r; r.x = __builtin_amdgcn_rcpf(d.x); r.y = __builtin_amdgcn_rcpf(d.y);
        const f32x2 o = (G * U) * r;
        return pack_bf16(o.x, o.y);
    };
    const unsigned gdst = WS_HH + (unsigned)((pn * 14 + wn * 7 + (g >> 1)) * CHS + (pm * 256 + 64 * wm + 16 * (g & 1) + l15) * 16);
    u32x2 zz[2];
#pragma unroll
    for (int mp = 0; mp < 2; ++mp) {
#pragma unroll
        for (int n = 0; n < 3; ++n) {
            u32x2 o[2];
#pragma unroll
            for (int i = 0; i < 2; ++i) {
                const int m = 2 * mp + i;
                const f32x4 gg = acc[m][n] - st[m][0] * cw[n];
                const f32x4 uu = acc[m][n + 3] - st[m][0] * cw[n + 3];
                o[i].x = geglu2(gg.xy, uu.xy, st[m]);
                o[i].y = geglu2(gg.zw, uu.zw, st[m]);
            }
            st128(c.rs, gdst + n * (2 * CHS) + mp * 512, widen(o[0], o[1]));
            fence();
        }
        const f32x4 y0 = acc[2 * mp][6] - st[2 * mp][0] * cw[6];
        const f32x4 y1 = acc[2 * mp + 1][6] - st[2 * mp + 1][0] * cw[6];
        const f32x4 sx = lane < 32 ? st[2 * mp] : st[2 * mp + 1];
        f32x4 gg, uu;
#pragma unroll
        for (int j = 0; j < 4; ++j) {
            auto r = __builtin_amdgcn_permlane32_swap(__float_as_uint(y0[j]), __float_as_uint(y1[j]), false, false);
            gg[j] = __uint_as_float(r[0]); uu[j] = __uint_as_float(r[1]);
        }
        zz[mp] = (u32x2){geglu2(gg.xy, uu.xy, sx), geglu2(gg.zw, uu.zw, sx)};
        STAMP(3, 3 + mp);
    }
    st128(c.rs, WS_HH + (unsigned)((pn * 14 + wn * 7 + 6) * CHS + (pm * 256 + 64 * wm + 16 * (2 * (g & 1) + (g >> 1)) + l15) * 16), widen(zz[0], zz[1]));
    STAMP(3, 5);
    wait_vm<0>();
    bar();
    if (tid == 0) st64(c.rs, FL_F6 + pm * 256 + pn * 8, c.tag);
    STAMP(3, 7);
}

template <int PHASES>
__global__ __launch_bounds__(512, 2) void fused_kernel(const u16* __restrict__ x, const u16* __restrict__ wqkv,
                                                       const u16* __restrict__ wo, const u16* __restrict__ wg,
                                                       const u16* __restrict__ wu, const u16* __restrict__ wd,
                                                       u16* __restrict__ out, char* __restrict__ ws) {
    constexpr bool MEGA = PHASES == 15;
    extern __shared__ __attribute__((aligned(16))) char smem_raw[];
    Ctx c;
    c.lds = (LAS char*)smem_raw;
    c.lds0 = (unsigned)(uintptr_t)c.lds;
    c.rs = __builtin_amdgcn_make_buffer_rsrc((void*)ws, 0, WS_END, 0x00020000);
    c.ws = ws;
    c.tid = threadIdx.x; c.lane = c.tid & 63;
    c.w = __builtin_amdgcn_readfirstlane(c.tid >> 6);
    c.bid = blockIdx.x;
    {
        asm volatile("buffer_load_dwordx2 %0, off, %1, 0 sc1" : "=v"(c.tag) : "s"(c.rs) : "memory");
        if constexpr (!(PHASES & 1)) finish_tag<0>(c);
    }
    if constexpr (PHASES & 1) phase1<MEGA>(c, x, wqkv, wo, wg, wu);
    if constexpr (PHASES & 2) phase2<MEGA>(c, wo, x, wg, wu, wd);
    if constexpr (PHASES & 4) phase3<MEGA>(c, wg, wu, wd);
    if constexpr (PHASES & 8) {
        phase4<MEGA>(c, wd, out);
        if (c.bid == 0 && c.tid == 0) st64(c.rs, FL_EPOCH, c.tag);
    }
}

template <int PHASES>
static void launch_phases(void* const* d_in, void* d_out, void* d_ws, hipStream_t stream) {
    (void)hipFuncSetAttribute((const void*)fused_kernel<PHASES>, hipFuncAttributeMaxDynamicSharedMemorySize, LDS_BYTES);
    fused_kernel<PHASES><<<256, 512, LDS_BYTES, stream>>>((const u16*)d_in[0], (const u16*)d_in[1], (const u16*)d_in[2],
                                                          (const u16*)d_in[4], (const u16*)d_in[5], (const u16*)d_in[6],
                                                          (u16*)d_out, (char*)d_ws);
}

extern "C" void kernel_launch(void* const* d_in, const int* in_sizes, int n_in, void* d_out, int out_size,
                              void* d_ws, size_t ws_size, hipStream_t stream) {
    launch_phases<15>(d_in, d_out, d_ws, stream);
}
```

```cpp
#include <hip/hip_runtime.h>
#include <hip/hip_bf16.h>
#include <stdint.h>
#include <type_traits>

#define LAS __attribute__((address_space(3)))
#define DEVI __device__ __forceinline__
#define IC(n) std::integral_constant<int, (n)>{}

typedef __attribute__((ext_vector_type(8))) __bf16 bf16x8;
typedef __attribute__((ext_vector_type(2))) __bf16 bf16x2;
typedef __attribute__((ext_vector_type(4))) short s16x4;
typedef __attribute__((ext_vector_type(8))) short s16x8;
typedef __attribute__((ext_vector_type(4))) float f32x4;
typedef __attribute__((ext_vector_type(2))) float f32x2;
typedef __attribute__((ext_vector_type(4))) unsigned u32x4;
typedef __attribute__((ext_vector_type(2))) unsigned u32x2;
typedef unsigned short u16;
typedef unsigned long long u64;
typedef __amdgpu_buffer_rsrc_t rsrc_t;

constexpr int SEQ = 2048, DM = 1024, NH = 32, DF = 3584;
constexpr float LN_EPS = 1e-5f;
constexpr int LDS_BYTES = 163840;
constexpr int SC1 = 16;

constexpr unsigned FL_EPOCH = 0;
constexpr unsigned FL_F1R = 1024;
constexpr unsigned FL_F1C = FL_F1R + 2048;
constexpr unsigned FL_F2 = FL_F1C + 2048;
constexpr unsigned FL_F3 = FL_F2 + 2048;
constexpr unsigned FL_F4 = FL_F3 + 2048;
constexpr unsigned FL_F6 = FL_F4 + 2048;
constexpr unsigned FL_F7 = FL_F6 + 2048;
#define CHPAD 256
constexpr int CHS = SEQ * 16 + CHPAD;
constexpr unsigned WS_O = 65536;
constexpr unsigned WS_H = WS_O + 128 * CHS;
constexpr unsigned WS_HH = WS_H + 128 * CHS;
constexpr unsigned KVS = 4352;
constexpr unsigned WS_KVP = WS_HH + 448 * CHS;
constexpr unsigned WS_RS1 = WS_KVP + 256 * KVS;
constexpr unsigned WS_CS1 = WS_RS1 + 16 * 16 * 128 * 8;
constexpr unsigned WS_ST2 = WS_CS1 + 16 * 16 * 192 * 4;
constexpr unsigned WS_CS3 = WS_ST2 + SEQ * 16 * 8;
constexpr unsigned WS_END = WS_CS3 + 4 * 7168 * 4;
constexpr unsigned SENT = 0xffffffffu;

DEVI unsigned pack_bf16(float lo, float hi) {
    bf16x2 v; v.x = (__bf16)lo; v.y = (__bf16)hi;
    return __builtin_bit_cast(unsigned, v);
}
DEVI float bf_lo(unsigned u) { return __uint_as_float(u << 16); }
DEVI float bf_hi(unsigned u) { return __uint_as_float(u & 0xffff0000u); }
DEVI float round_bf16(float x) { return (float)(__bf16)x; }

template <int OFF, int POL = 0> DEVI void glds(const char* sbase, unsigned voff, unsigned lds_dst) {
    if constexpr (POL == 0)
        asm volatile("s_mov_b32 m0, %2\n\ts_nop 0\n\tglobal_load_lds_dwordx4 %0, %1 offset:%3 sc1"
                     : : "v"(voff), "s"(sbase), "s"(lds_dst), "n"(OFF) : "memory");
    else if constexpr (POL == 1)
        asm volatile("s_mov_b32 m0, %2\n\ts_nop 0\n\tglobal_load_lds_dwordx4 %0, %1 offset:%3 nt"
                     : : "v"(voff), "s"(sbase), "s"(lds_dst), "n"(OFF) : "memory");
    else
        asm volatile("s_mov_b32 m0, %2\n\ts_nop 0\n\tglobal_load_lds_dwordx4 %0, %1 offset:%3"
                     : : "v"(voff), "s"(sbase), "s"(lds_dst), "n"(OFF) : "memory");
}
#define ROT2 0
#define DEP2 4
#define WDT 1
#define POL1 2
#define POL0 2
#define POL2 2
DEVI bf16x8 lds_b128(LAS char* p) { return *(LAS bf16x8*)p; }
DEVI bf16x8 lds_tr2(LAS char* p0, LAS char* p1) {
    s16x4 a = __builtin_amdgcn_ds_read_tr16_b64_v4i16((LAS s16x4*)p0);
    s16x4 b = __builtin_amdgcn_ds_read_tr16_b64_v4i16((LAS s16x4*)p1);
    s16x8 r = __builtin_shufflevector(a, b, 0, 1, 2, 3, 4, 5, 6, 7);
    return __builtin_bit_cast(bf16x8, r);
}
DEVI f32x4 mfma16(bf16x8 a, bf16x8 b, f32x4 c) { return __builtin_amdgcn_mfma_f32_16x16x32_bf16(a, b, c, 0, 0, 0); }
template <int N> DEVI void wait_vm() { asm volatile("s_waitcnt vmcnt(%0)" ::"n"(N) : "memory"); }
DEVI void wait_lgkm0() { asm volatile("s_waitcnt lgkmcnt(0)" ::: "memory"); }
DEVI void bar() {
    asm volatile("" ::: "memory");
    __builtin_amdgcn_s_barrier();
    asm volatile("" ::: "memory");
}
DEVI void fence() { __builtin_amdgcn_sched_barrier(0); }
DEVI float xor16_32_sum(float v) {
    auto a = __builtin_amdgcn_permlane16_swap(__float_as_uint(v), __float_as_uint(v), false, false);
    v = __uint_as_float(a[0]) + __uint_as_float(a[1]);
    auto b = __builtin_amdgcn_permlane32_swap(__float_as_uint(v), __float_as_uint(v), false, false);
    return __uint_as_float(b[0]) + __uint_as_float(b[1]);
}
DEVI f32x4 phi4(f32x4 t2) {
    f32x4 e, m;
#pragma unroll
    for (int j = 0; j < 4; ++j) { e[j] = __builtin_amdgcn_fmed3f(__builtin_amdgcn_exp2f(t2[j]), 0.f, 1.f); m[j] = fmaxf(t2[j], 0.f); }
    return (m * 0.6931471805599453f + e) + 1e-6f;
}
constexpr float LOG2E = 1.4426950408889634f;
DEVI bf16x8 ones8() {
    bf16x8 o;
#pragma unroll
    for (int j = 0; j < 8; ++j) o[j] = (__bf16)1.0f;
    return o;
}
DEVI void combine8(const f32x4 (&v)[4], float& mean, float& m2) {
    const float ms = ((v[0][0] + v[0][2]) + (v[1][0] + v[1][2])) + ((v[2][0] + v[2][2]) + (v[3][0] + v[3][2]));
    mean = ms * 0.125f;
    float dd = 0.f, q = 0.f;
#pragma unroll
    for (int i = 0; i < 4; ++i) {
        const float d0 = v[i][0] - mean, d1 = v[i][2] - mean;
        dd += d0 * d0 + d1 * d1;
        q += v[i][1] + v[i][3];
    }
    m2 = q + 64.f * dd;
}
DEVI u32x4 ld128(rsrc_t rs, unsigned off) { return __builtin_amdgcn_raw_buffer_load_b128(rs, off, 0, SC1); }
DEVI void st128(rsrc_t rs, unsigned off, u32x4 v) { __builtin_amdgcn_raw_buffer_store_b128(v, rs, off, 0, SC1); }
DEVI void st64(rsrc_t rs, unsigned off, u32x2 v) { __builtin_amdgcn_raw_buffer_store_b64(v, rs, off, 0, SC1); }
DEVI void st32(rsrc_t rs, unsigned off, unsigned v) { __builtin_amdgcn_raw_buffer_store_b32(v, rs, off, 0, SC1); }
DEVI u32x2 ld64(rsrc_t rs, unsigned off) { return __builtin_amdgcn_raw_buffer_load_b64(rs, off, 0, SC1); }
DEVI unsigned ld32(rsrc_t rs, unsigned off) { return __builtin_amdgcn_raw_buffer_load_b32(rs, off, 0, SC1); }
DEVI u32x4 no_sent(f32x4 v) {
    u32x4 u = __builtin_bit_cast(u32x4, v);
#pragma unroll
    for (int j = 0; j < 4; ++j) u[j] = min(u[j], SENT - 1u);
    return u;
}
DEVI unsigned umax4(u32x4 v) { return max(max(v[0], v[1]), max(v[2], v[3])); }
DEVI u32x4 widen(u32x2 a, u32x2 b) {
    auto r0 = __builtin_amdgcn_permlane16_swap(a.x, b.x, false, false);
    auto r1 = __builtin_amdgcn_permlane16_swap(a.y, b.y, false, false);
    return (u32x4){r0[0], r1[0], r0[1], r1[1]};
}
DEVI u32x2 pack4(f32x4 v) { return (u32x2){pack_bf16(v[0], v[1]), pack_bf16(v[2], v[3])}; }
DEVI bool timed_out(u64 t0) { return __builtin_amdgcn_s_memrealtime() - t0 > 1000000ull; }

DEVI void wait_flags(rsrc_t rs, unsigned off, u32x2 tag) {
    const u64 t0 = __builtin_amdgcn_s_memrealtime();
    for (;;) {
        const u32x2 v = ld64(rs, off);
        if (__all(v.x == tag.x && v.y == tag.y)) break;
        if (__builtin_amdgcn_s_memrealtime() - t0 > 1000000ull) break;
        __builtin_amdgcn_s_sleep(1);
    }
    asm volatile("" ::: "memory");
}

#define STAMP(kid, i)

struct Ctx {
    LAS char* lds;
    unsigned lds0;
    rsrc_t rs;
    const char* ws;
    u32x2 tag;
    int tid, lane, w, bid;
    int bad;
};
DEVI void fresh_ids(Ctx& c) { asm volatile("" : "+v"(c.tid)); c.lane = c.tid & 63; }
template <int N> DEVI void finish_tag(Ctx& c) {
    asm volatile("s_waitcnt vmcnt(%1)" : "+v"(c.tag) : "n"(N) : "memory");
    const unsigned ex = __builtin_amdgcn_readfirstlane(c.tag.x), ey = __builtin_amdgcn_readfirstlane(c.tag.y);
    c.tag.x = ex + 1u;
    c.tag.y = ey ^ 0x9e3779b9u;
}
template <int STRIDE> DEVI void asm_load4(rsrc_t rs, unsigned voff, f32x4 (&v)[4]) {
    asm volatile("buffer_load_dwordx4 %0, %4, %5, 0 offen sc1\n\tbuffer_load_dwordx4 %1, %4, %5, %6 offen sc1\n\t"
                 "buffer_load_dwordx4 %2, %4, %5, %7 offen sc1\n\tbuffer_load_dwordx4 %3, %4, %5, %8 offen sc1"
                 : "=&v"(v[0]), "=&v"(v[1]), "=&v"(v[2]), "=&v"(v[3])
                 : "v"(voff), "s"(rs), "s"(STRIDE), "s"(2 * STRIDE), "s"(3 * STRIDE)
                 : "memory");
}
DEVI void asm_load1(rsrc_t rs, unsigned voff, int soff, u32x4& v) {
    asm volatile("buffer_load_dwordx4 %0, %1, %2, %3 offen sc1" : "=v"(v) : "v"(voff), "s"(rs), "s"(soff) : "memory");
}
template <int N> DEVI void wait_regs2(u32x4 (&v)[2]) {
    asm volatile("s_waitcnt vmcnt(%2)" : "+v"(v[0]), "+v"(v[1]) : "n"(N) : "memory");
}
template <int N> DEVI void wait_regs4(f32x4 (&v)[4]) {
    asm volatile("s_waitcnt vmcnt(%4)" : "+v"(v[0]), "+v"(v[1]), "+v"(v[2]), "+v"(v[3]) : "n"(N) : "memory");
}

namespace g2 { constexpr int B_RING = 65536, B_PIECE = 8192; }
namespace g4 { constexpr int B_RING = 81920, B_PIECE = 16384; }
namespace g3 {
constexpr int A_PIECE = 16384, B_SUB = 32 * 224, B_PIECE = 2 * B_SUB;
constexpr int NR = 5;
constexpr int B_OFF = NR * A_PIECE;
constexpr int TAB_OFF = B_OFF + NR * B_PIECE;
constexpr int SCR_OFF = TAB_OFF + 4096 + 1024;
constexpr int OPITCH = 240;
constexpr unsigned BSTEP = 32 * DF * 2, AHALF = 128 * 16, ASTEP = 8 * CHS;
}

DEVI unsigned inbox_of(int b) { return WS_O + (unsigned)((b & 1) * 4 + ((b >> 3) & 3)) * 4096u + (unsigned)(((b & 7) >> 1) * 8 + (b >> 5)) * (4u * CHS); }

template <bool G4> struct RsGeom {
    int pm, pn, ks;
    unsigned voffB[2];
    const char* gB;
    DEVI RsGeom(const Ctx& c, const u16* W) {
        const int xcd = c.bid & 7, loc = c.bid >> 3;
        if constexpr (G4) {
            ks = xcd & 1; pm = (xcd >> 1) * 4 + (loc & 3); pn = loc >> 2;
#pragma unroll
            for (int j = 0; j < 2; ++j) {
                const int row = 4 * (2 * c.w + j) + (c.lane >> 4), ch = c.lane & 15;
                const int s = (row & 3) | (((row >> 3) & 1) << 2);
                voffB[j] = (unsigned)(row * 2048 + pn * 256 + ((ch ^ (s << 1)) * 16) - j * 1024);
            }
            gB = (const char*)W + (size_t)ks * 1792 * 2048;
        } else {
            ks = 0; pm = (xcd & 3) * 4 + (loc & 3); pn = (xcd >> 2) * 8 + (loc >> 2);
            const int row = 8 * c.w + (c.lane >> 3), cb = c.lane & 7;
            const int f = ((row >> 1) & 1) | (((row >> 3) & 1) << 1);
            voffB[0] = (unsigned)(row * 2048 + pn * 128 + ((cb ^ (f << 1)) * 16));
            voffB[1] = 0;
            gB = (const char*)W;
        }
    }
    DEVI void pieceB(const Ctx& c, int p) const {
        const char* b = gB + (size_t)p * (64 * 2048);
        if constexpr (G4) {
            const unsigned d = c.lds0 + g4::B_RING + (p & 3) * g4::B_PIECE + 2 * c.w * 1024;
            glds<0>(b, voffB[0], d); glds<1024>(b, voffB[1], d);
        } else {
            glds<0, POL2>(b, voffB[0], c.lds0 + (p < 12 ? g2::B_RING + p * g2::B_PIECE : (p - 12) * g2::B_PIECE) + c.w * 1024);
        }
    }
};

struct G3Geom {
    int pm, pn;
    unsigned voffB[2];
    const char* gB[2];
    unsigned ldsB, ringmask;
    DEVI G3Geom(const Ctx& c, const u16* wg, const u16* wu, const u16* wd) {
        using namespace g3;
        const int xcd = c.bid & 7, loc = c.bid >> 3;
        pm = (xcd & 1) * 4 + (loc & 3); pn = (xcd >> 1) * 8 + (loc >> 2);
        if (c.w < 7) {
#pragma unroll
            for (int j = 0; j < 2; ++j) {
                const int b = 2 * c.w + j, sub = b >= 7 ? 1 : 0;
                const int i = (b - 7 * sub) * 64 + c.lane, rho = i / 14, cc = i % 14;
                const int r = (rho & ~12) | ((rho & 4) << 1) | ((rho & 8) >> 1);
                voffB[j] = (unsigned)(r * DF * 2 + (pn * 112 + cc * 8) * 2);
                gB[j] = (const char*)(sub ? wu : wg) - j * 1024;
            }
            ldsB = c.lds0 + B_OFF + 2 * c.w * 1024;
            ringmask = ~0u;
        } else {
            const int j = loc >> 2, l0 = min(c.lane & 15, 13);
            const bool wide = ((j + 1) & 2) != 0;
            const int ti = ((j >> 1) * 4 + (loc & 3)) * 8 + xcd;
            voffB[0] = wide ? (unsigned)(pn * 224) : (unsigned)((ti * 14 + l0) * 128);
            voffB[1] = voffB[0] + (WDT == 1 ? 64 : 0);
            gB[0] = (const char*)(wide ? wg : wd); gB[1] = gB[0] - 1024;
            ldsB = c.lds0 + SCR_OFF;
            ringmask = 0u;
        }
    }
    DEVI void prefetchB(const Ctx& c) const {
#pragma unroll
        for (int p = 0; p < 4; ++p) {
            const unsigned d = ldsB + ((unsigned)(p * g3::B_PIECE) & ringmask);
            glds<0>(gB[0] + (size_t)p * g3::BSTEP, voffB[0], d);
            glds<1024>(gB[1] + (size_t)p * g3::BSTEP, voffB[1], d);
        }
    }
};

namespace g1 {
constexpr int A_BYTES = 16384, B_PITCH = 384, STAGE = 40960;
constexpr int TAB = 40960, CRED = 43008, KVT = 47104;
#define TCHMODE 0
#define NT0 0
#define TCH0 6
#define BARI 1
#define CUP 2
#define HELP 1
#define CVPOS 0
#define P3LATE 1
#define OWNT 1
#define NOFF 4
constexpr int NST = 16 / NOFF;
#define FLT (NOFF == 4 ? 9 : 11)
}

template <bool MEGA>
DEVI void phase1(Ctx& c, const u16* __restrict__ x, const u16* __restrict__ wqkv, const u16* __restrict__ wo,
                 const u16* __restrict__ wg, const u16* __restrict__ wu) {
    using namespace g1;
    STAMP(0, 0);
    LAS char* lds = c.lds;
    const int tid = c.tid, lane = c.lane, w = c.w;
    const int wm = w >> 1, wn = w & 1;
    const int xcd = c.bid & 7, loc = c.bid >> 3;
    const int pm = (xcd & 1) * 8 + (loc & 7);
    const int hp = (xcd >> 1) * 4 + (loc >> 3);
    const int s0 = 4 * (((xcd >> 1) + 2 * (xcd & 1) + ((loc & 7) >> 2)) & 3), sidx = ((loc & 3) + (loc >> 3)) & 3;
    const int l15 = lane & 15, g = lane >> 4, q4 = (lane >> 2) & 3, p4 = lane & 3;

    unsigned voff[6], dst[6];
#pragma unroll
    for (int k = 0; k < 6; ++k) {
        const int i = w + 7 * k < 40 ? w + 7 * k : w + 7 * k - 7;
        const int rowa = 8 * i + (lane >> 3), ch = (lane & 7) ^ ((rowa >> 1) & 7);
        const int ib = (i - 16) * 64 + lane;
        const int row = ib / 24, cc = ib % 24, blk = cc >> 3, cb = cc & 7;
        const int f = ((row >> 1) & 1) | (((row >> 3) & 1) << 1);
        const unsigned oa = (unsigned)(rowa * DM * 2 + ch * 16);
        const unsigned ob = (unsigned)(row * (3 * DM) * 2 + (blk * DM + hp * 64 + (cb ^ (f << 1)) * 8) * 2);
        voff[k] = k < 2 ? oa : (k > 2 ? ob : (i < 16 ? oa : ob));
        dst[k] = c.lds0 + i * 1024;
    }
    const char* gA = (const char*)x + (size_t)pm * 128 * DM * 2;
    const char* gB = (const char*)wqkv;
    auto issue = [&](int s, int k) {
        const int ck = (s0 + s) & 15;
        const char* pa = gA + ck * 128;
        const char* pb = gB + (size_t)ck * (64 * 3 * DM * 2);
        if (s < NT0) glds<0, 1>(k < 2 ? pa : (k > 2 ? pb : (w < 2 ? pa : pb)), voff[k], dst[k] + (unsigned)((s & 3) * STAGE));
        else glds<0, POL1>(k < 2 ? pa : (k > 2 ? pb : (w < 2 ? pa : pb)), voff[k], dst[k] + (unsigned)((s & 3) * STAGE));
    };
    if (w < 7) {
#pragma unroll
        for (int k = 0; k < 6; ++k) issue(0, k);
    }
    bar();
    if (w < 7) {
#pragma unroll
        for (int k = 0; k < 6; ++k) issue(1, k);
    }
    const unsigned kvslot = WS_KVP + (hp * 16 + pm) * KVS;
    {
        const u32x4 mk = {SENT, SENT, SENT, SENT};
        st128(c.rs, tid < 272 ? kvslot + tid * 16 : 0xfffffff0u, mk);
    }
    unsigned touch = 0, touchA = 0, voffH;
    const char* gH;
    {
        const int jb = (3 - ((int)((uintptr_t)wqkv >> 7) + (hp & 12))) & 7, dj = ((hp & 3) - jb) & 3;
        const bool helper = HELP && jb < 4 && dj != 0;
        const int c0 = 4 * (((xcd >> 1) + 2 * (xcd & 1)) & 3) + (loc & 7);
        voffH = helper ? (unsigned)(lane * (3 * DM * 2) + (dj - 1) * (DM * 2) + ((hp & 12) + jb) * 128) : 0u;
        gH = gB + (size_t)(c0 & 15) * (64 * 3 * DM * 2);
        if (w == 7) asm volatile("global_load_dword %0, %1, %2" : "+v"(touchA) : "v"(voffH), "s"(gH) : "memory");
        gH = gB + (size_t)((c0 + 8) & 15) * (64 * 3 * DM * 2);
    }
    fence();

    const int fB = ((q4 >> 1) & 1) | ((g & 1) << 1);
    int va[4][2], vb[4][2];
#pragma unroll
    for (int s = 0; s < 4; ++s)
#pragma unroll
        for (int i = 0; i < 2; ++i) {
            va[s][i] = s * STAGE + (32 * wm + l15) * 128 + 16 * ((4 * i + g) ^ (l15 >> 1));
            vb[s][i] = s * STAGE + A_BYTES + (8 * g + q4) * B_PITCH + 32 * ((2 * wn + i) ^ fB) + 8 * p4;
        }
    f32x4 acc[2][6];
#pragma unroll
    for (int m = 0; m < 2; ++m)
#pragma unroll
        for (int n = 0; n < 6; ++n) acc[m][n] = (f32x4){0.f, 0.f, 0.f, 0.f};

    const unsigned floff = lane < 16 ? FL_F1R + pm * 128 + l15 * 8 : FL_F1C + hp * 128 + l15 * 8;
    const unsigned vlin = (unsigned)lane * 16u;
    auto issue_stats = [&](int k) {
        const int i = w + 7 * k;
        glds<0>(c.ws + (i < 16 ? WS_RS1 + pm * 16384 + i * 1024 : WS_CS1 + hp * 12288 + (i - 16) * 1024), vlin, dst[k]);
    };
    const bool slow = (hp & 7) == c.bad;
    const int cid = min(hp - (hp > c.bad) - (hp > c.bad + 8), 13) * 16 + pm, cg = cid % 56, rr = cid / 56;
    const char* gC = (const char*)(cg < 28 ? wg : wu) + (size_t)(rr * 256 + w * 32) * (DF * 2) + (cg < 28 ? cg : cg - 28) * 256;
    const unsigned voffC = slow ? 0u : (unsigned)(g * (DF * 2) + l15 * 16);
    u32x4 cv[8];
    auto cv_load = [&](int j) {
        asm volatile("global_load_dwordx4 %0, %1, %2" : "=v"(cv[j]) : "v"(voffC), "s"(gC + (size_t)j * (4 * DF * 2)) : "memory");
    };
    const char* gT = (const char*)(cid < 112 ? wg : wu) + (size_t)(cid % 112) * 65536;
    const unsigned voffT = slow ? 0u : 8 * vlin;
    constexpr int TSTEP = 8192, NT = 8;
    const RsGeom<false> gn(c, wo);

    LAS float* rowmu = (LAS float*)(lds + TAB);
    LAS float* rowr = (LAS float*)(lds + TAB + 512);
    LAS float* coltab = (LAS float*)(lds + TAB + 1024);
    LAS unsigned* verdict = (LAS unsigned*)(lds + TAB + 1792);
    auto tables = [&]() {
        if (tid < 256) {
            const int row = tid >> 1, hf = tid & 1;
            f32x4 v[4];
#pragma unroll
            for (int i = 0; i < 4; ++i) {
                const f32x2 p0 = *(LAS f32x2*)(lds + ((8 * hf + 2 * i) * 128 + row) * 8);
                const f32x2 p1 = *(LAS f32x2*)(lds + ((8 * hf + 2 * i + 1) * 128 + row) * 8);
                v[i] = (f32x4){p0.x, p0.y, p1.x, p1.y};
            }
            float mean, m2;
            combine8(v, mean, m2);
            const float mo = __shfl_xor(mean, 1), m2o = __shfl_xor(m2, 1);
            const float d = mean - mo;
            const float var = (m2 + m2o + 256.f * d * d) * (1.f / DM);
            if (hf == 0) rowmu[row] = 0.5f * (mean + mo); else rowr[row] = rsqrtf(var + LN_EPS);
        } else if (tid < 448) {
            const int col = tid - 256;
            float s = 0.f;
#pragma unroll
            for (int i = 0; i < 16; ++i) s += *(LAS float*)(lds + A_BYTES + (i * 192 + col) * 4);
            coltab[col] = s;
        }
    };

    bf16x8 fa[2][2], fb[2][6];
    auto ldA = [&](int s, int kk, int m) { return lds_b128(lds + va[s][kk] + m * 2048); };
    auto ldB = [&](int s, int kk, int n) {
        LAS char* pb = lds + vb[s][n & 1] + kk * 32 * B_PITCH + (n >> 1) * 128;
        return lds_tr2(pb, pb + 4 * B_PITCH);
    };
    const int tstart = sidx > TCH0 ? sidx : TCH0;
    const int f1step = w == 0 ? sidx + 2 : -1;
    const unsigned f1off = lane > 1 ? 0xfffffff0u : (lane ? FL_F1C + hp * 128 + pm * 8 : FL_F1R + pm * 128 + hp * 8);
    auto stats = [&](auto s_tag) {
        constexpr int SB = decltype(s_tag)::value;
        const int sc = s0 + sidx;
        const int row = 16 * w + l15;
        const u32x4 r0 = __builtin_bit_cast(u32x4, lds_b128(lds + SB * STAGE + row * 128 + 16 * (g ^ (l15 >> 1))));
        const u32x4 r1 = __builtin_bit_cast(u32x4, lds_b128(lds + SB * STAGE + row * 128 + 16 * ((4 + g) ^ (l15 >> 1))));
        const int part = wm == 3 ? 0 : wm;
        f32x4 cs[2];
#pragma unroll
        for (int i = 0; i < 2; ++i) {
            cs[i] = (f32x4){0.f, 0.f, 0.f, 0.f};
#pragma unroll
            for (int kh = 0; kh < 2; ++kh) {
                LAS char* pb = lds + vb[SB][i] + kh * 32 * B_PITCH + part * 128;
                cs[i] = mfma16(ones8(), lds_tr2(pb, pb + 4 * B_PITCH), cs[i]);
            }
        }
        float v[16];
#pragma unroll
        for (int j = 0; j < 4; ++j) { v[2 * j] = bf_lo(r0[j]); v[2 * j + 1] = bf_hi(r0[j]); v[8 + 2 * j] = bf_lo(r1[j]); v[9 + 2 * j] = bf_hi(r1[j]); }
        float s = 0.f;
#pragma unroll
        for (int j = 0; j < 16; ++j) s += v[j];
        const float mu = xor16_32_sum(s) * (1.f / 64.f);
        float m2 = 0.f;
#pragma unroll
        for (int j = 0; j < 16; ++j) { const float d = v[j] - mu; m2 += d * d; }
        m2 = xor16_32_sum(m2);
        st64(c.rs, WS_RS1 + ((pm * 16 + sc) * 128 + row) * 8, (u32x2){__float_as_uint(mu), __float_as_uint(m2)});
        st32(c.rs, WS_CS1 + ((hp * 16 + sc) * 192 + part * 64 + wn * 32 + (g & 1) * 16 + l15) * 4, __float_as_uint((g & 1) ? cs[1][0] : cs[0][0]));
        wait_lgkm0();
    };
    auto mainloop = [&](auto ld_tag) {
        constexpr bool LOADER = decltype(ld_tag)::value;
        u32x2 fl;
        auto vm_slot = [&](auto t_tag, int k) {
            constexpr int T = decltype(t_tag)::value;
            if constexpr (LOADER) {
                if constexpr (T == FLT) {
                    if (k == 0) asm volatile("buffer_load_dwordx2 %0, %1, %2, 0 offen sc1" : "=v"(fl) : "v"(floff), "s"(c.rs) : "memory");
                }
                if constexpr (T == -1) { issue(2, 2 * (k - 3)); issue(2, 2 * (k - 3) + 1); }
                if constexpr (T >= 0 && T < CUP) issue(T + 3, k);
                if constexpr (T == CUP) { issue(T + 3 + k / 3, 2 * (k % 3)); issue(T + 3 + k / 3, 2 * (k % 3) + 1); }
                if constexpr (T > CUP && T < 12) issue(T + 4, k);
                if constexpr (T == 12) { if (k < 4) issue_stats(k); }
            } else {
                if constexpr (T == 1) {
                    if (k == 0) asm volatile("global_load_dword %0, %1, %2" : "+v"(touch) : "v"(voffH), "s"(gH) : "memory");
                }
                if constexpr (T >= TCH0 && T < (NST - 1 > TCH0 ? NST - 1 : TCH0) + NT + 1) {
                    const unsigned n = (unsigned)(T - tstart);
                    if (k == 0) {
                        const char* tp = n == 0 ? (const char*)wo + c.bid * 8192 : gT + (int)(n - 1) * TSTEP;
                        asm volatile("s_cmp_gt_u32 %3, %4\n\ts_cbranch_scc1 1f\n\tglobal_load_dword %0, %1, %2\n1:"
                                     : "+v"(touch) : "v"(n == 0 ? 8 * vlin : voffT), "s"(tp), "s"(n), "n"(NT) : "memory", "scc");
                    }
                    if constexpr (T < NST || T == TCH0) {
                        if (k == 1)
                            asm volatile("s_cmp_lg_u32 %3, 0\n\ts_cbranch_scc1 1f\n\tglobal_load_dword %0, %1, %2\n1:"
                                         : "+v"(touch) : "v"(0u), "s"(c.ws + c.bid * 128), "s"(n) : "memory", "scc");
                    }
                }
            }
            if constexpr (T == 13 && MEGA) { if (k < 2) gn.pieceB(c, k); }
            if constexpr (T == 14 && MEGA) { if (k < 5) gn.pieceB(c, 2 + k); }
            if constexpr (T == 15 && MEGA) { gn.pieceB(c, 7 + 2 * k); if (k < 2) gn.pieceB(c, 8 + 2 * k); }
        };
        auto half = [&](auto t_tag, auto kk_tag) {
            constexpr int t = decltype(t_tag)::value, kk = decltype(kk_tag)::value;
            constexpr int nt = kk ? t + 1 : t, nk = kk ^ 1, ns = nt & 3;
#pragma unroll
            for (int i = 0; i < 12; ++i) {
                const int m = i / 6, n = i % 6;
                if (n < 2) acc[m][n] = mfma16(fb[kk][n], fa[kk][m], acc[m][n]);
                else acc[m][n] = mfma16(fa[kk][m], fb[kk][n], acc[m][n]);
                if constexpr (nt < 16) {
                    if (i == 0) { fa[nk][0] = ldA(ns, nk, 0); fa[nk][1] = ldA(ns, nk, 1); }
                    else if (i <= 6) fb[nk][i - 1] = ldB(ns, nk, i - 1);
                }
                fence();
                if (kk == 1 && (i & 3) == 3) vm_slot(IC(t), i >> 2);
                if (kk == 0 && (i & 3) == 3) vm_slot(IC(t - 1), 3 + (i >> 2));
                fence();
            }
        };
        auto step = [&](auto t_tag, auto wv_tag) {
            constexpr int t = decltype(t_tag)::value, WV = decltype(wv_tag)::value;
            half(t_tag, IC(0));
            if constexpr (LOADER) { if constexpr (WV >= 0) wait_vm<WV>(); }
            else if constexpr (t >= 2 && t < NST + 2) {
                constexpr int ts = t - 2 > TCH0 ? t - 2 : TCH0;
                constexpr int cnt = (t - 2 >= ts ? 3 : (t - 1 >= ts ? 2 : 0)) + (t == 2 || t == 3 ? 1 : 0);
                asm volatile("s_cmp_lg_u32 %0, %1\n\ts_cbranch_scc1 1f\n\ts_waitcnt vmcnt(%2)\n1:" : : "s"(sidx), "n"(t - 2), "n"(cnt) : "memory", "scc");
            }
            if constexpr (!LOADER && t == NST + 1) asm volatile("" : "+v"(touchA));
            wait_lgkm0();
            fence();
            if constexpr (t < NST) { if (sidx == t) stats(IC(t & 3)); }
            fence();
            bar();
            fence();
            if constexpr (LOADER && t >= 2 && t < NST + 2) {
                asm volatile("s_cmp_lg_u32 %3, %4\n\ts_cbranch_scc1 1f\n\tbuffer_store_dwordx2 %0, %1, %2, 0 offen sc1\n1:"
                             : : "v"(c.tag), "v"(f1off), "s"(c.rs), "s"(f1step), "n"(t) : "memory", "scc");
                fence();
            }
            if constexpr (t == 13) {
                bool ok = true;
                if constexpr (LOADER) {
                    asm volatile("" : "+v"(fl));
                    ok = __all(fl.x == c.tag.x && fl.y == c.tag.y);
                }
                if (lane == 0) verdict[w] = ok ? 0u : 1u;
                fence();
            }
            if constexpr (LOADER && t == 14) { tables(); fence(); }
            half(t_tag, IC(1));
        };
#define WVS(t) IC((t) <= CUP ? 6 : 12)
        step(IC(0), IC(7)); step(IC(1), WVS(1)); step(IC(2), WVS(2)); step(IC(3), WVS(3));
        step(IC(4), WVS(4)); step(IC(5), WVS(5)); step(IC(6), WVS(6)); step(IC(7), IC(12));
        step(IC(8), IC(12)); step(IC(9), IC(12)); step(IC(10), IC(12)); step(IC(11), IC(12));
        step(IC(12), IC(FLT == 11 ? 6 : 12)); step(IC(13), IC(10)); step(IC(14), IC(MEGA ? 2 : 0)); step(IC(15), IC(-1));
    };

    if (w < 7) wait_vm<7>(); else wait_vm<2>();
    finish_tag<13>(c);
    bar();
    STAMP(0, 1);
    fa[0][0] = ldA(0, 0, 0); fa[0][1] = ldA(0, 0, 1);
#pragma unroll
    for (int n = 0; n < 6; ++n) fb[0][n] = ldB(0, 0, n);
    fence();
    if (w < 7) mainloop(std::true_type{}); else mainloop(std::false_type{});
    STAMP(0, 2);
    {
        const u32x4 v0 = *(LAS u32x4*)verdict, v1 = *(LAS u32x4*)(verdict + 4);
        if (__builtin_amdgcn_readfirstlane(umax4(v0) | umax4(v1))) {
            wait_flags(c.rs, floff, c.tag);
            if (w < 7) {
#pragma unroll
                for (int j = 0; j < 4; ++j) issue_stats(j);
            }
            wait_vm<0>();
            bar();
            tables();
            wait_lgkm0();
            bar();
        }
    }
    const int rbase = 32 * wm;
    bf16x8 pk[2], pv[2];
    {
        f32x4 mu[2], rs[2], rl[2];
#pragma unroll
        for (int mm = 0; mm < 2; ++mm) {
            mu[mm] = *(LAS f32x4*)(rowmu + rbase + 16 * mm + 4 * g);
            rs[mm] = *(LAS f32x4*)(rowr + rbase + 16 * mm + 4 * g);
            rl[mm] = rs[mm] * LOG2E;
        }
#pragma unroll
        for (int i = 0; i < 2; ++i) {
            const float cwk = coltab[64 + wn * 32 + i * 16 + l15], cwv = coltab[128 + wn * 32 + i * 16 + l15];
            u32x2 k2[2], v2[2];
#pragma unroll
            for (int mm = 0; mm < 2; ++mm) {
                k2[mm] = pack4(phi4((acc[mm][2 + i] - mu[mm] * cwk) * rl[mm]));
                v2[mm] = pack4((acc[mm][4 + i] - mu[mm] * cwv) * rs[mm]);
            }
            pk[i] = __builtin_bit_cast(bf16x8, (u32x4){k2[0].x, k2[0].y, k2[1].x, k2[1].y});
            pv[i] = __builtin_bit_cast(bf16x8, (u32x4){v2[0].x, v2[0].y, v2[1].x, v2[1].y});
        }
    }
    LAS float* red = (LAS float*)lds;
    {
        LAS float* mine = red + w * 1056;
#pragma unroll
        for (int i = 0; i < 2; ++i) {
#pragma unroll
            for (int i2 = 0; i2 < 2; ++i2) {
                const f32x4 d = mfma16(pk[i], pv[i2], (f32x4){0.f, 0.f, 0.f, 0.f});
                *(LAS f32x4*)(mine + (16 * i2 + l15) * 32 + 4 * ((4 * i + g) ^ (l15 & 7))) = d;
            }
            const f32x4 z = mfma16(pk[i], ones8(), (f32x4){0.f, 0.f, 0.f, 0.f});
            if (l15 == 0) *(LAS f32x4*)(mine + 1024 + 16 * i + 4 * g) = z;
        }
    }
    wait_lgkm0();
    bar();
    STAMP(0, 3);
    const int hh = tid >> 8, e4 = tid & 255;
    {
        const int es = (e4 ^ ((e4 >> 3) & 7)) * 4;
        f32x4 s = *(LAS f32x4*)(red + (hh + 0) * 1056 + es);
        s += *(LAS f32x4*)(red + (hh + 2) * 1056 + es);
        s += *(LAS f32x4*)(red + (hh + 4) * 1056 + es);
        s += *(LAS f32x4*)(red + (hh + 6) * 1056 + es);
        const u32x2 me = pack4(s);
        const u32x4 two = {me.x, me.y, (unsigned)__builtin_amdgcn_mov_dpp((int)me.x, 0xb1, 0xf, 0xf, true),
                           (unsigned)__builtin_amdgcn_mov_dpp((int)me.y, 0xb1, 0xf, 0xf, true)};
        if (!(tid & 1)) st128(c.rs, kvslot + tid * 8, no_sent(__builtin_bit_cast(f32x4, two)));
        if (tid < 16) {
            const int h2 = tid >> 3, z4 = 1024 + (tid & 7) * 4;
            f32x4 z = *(LAS f32x4*)(red + (h2 + 0) * 1056 + z4);
            z += *(LAS f32x4*)(red + (h2 + 2) * 1056 + z4);
            z += *(LAS f32x4*)(red + (h2 + 4) * 1056 + z4);
            z += *(LAS f32x4*)(red + (h2 + 6) * 1056 + z4);
            st128(c.rs, kvslot + 4096 + tid * 16, no_sent(z));
        }
    }
    fence();
#pragma unroll
    for (int j = 0; j < 8; ++j) cv_load(j);
    fence();
    bf16x8 pq[2];
#pragma unroll
    for (int mm = 0; mm < 2; ++mm) {
        const float mu = rowmu[rbase + 16 * mm + l15], rl = rowr[rbase + 16 * mm + l15] * LOG2E;
        u32x2 q2[2];
#pragma unroll
        for (int i = 0; i < 2; ++i) {
            const f32x4 cw = *(LAS f32x4*)(coltab + wn * 32 + i * 16 + 4 * g);
            q2[i] = pack4(phi4((acc[mm][i] - mu * cw) * rl));
        }
        pq[mm] = __builtin_bit_cast(bf16x8, (u32x4){q2[0].x, q2[0].y, q2[1].x, q2[1].y});
    }
    asm volatile("" : "+v"(pq[0]), "+v"(pq[1]));
    STAMP(0, 4);
    LAS u16* kvt = (LAS u16*)(lds + KVT);
    {
        const unsigned src = WS_KVP + hp * 16 * KVS;
        const u64 t0 = __builtin_amdgcn_s_memrealtime();
        for (;;) {
            const unsigned v = ld32(c.rs, src + l15 * KVS + w * 512);
            if (__all(v != SENT) || timed_out(t0)) break;
            __builtin_amdgcn_s_sleep(1);
        }
        STAMP(0, 5);
        asm volatile("s_waitcnt vmcnt(0)"
                     : "+v"(cv[0]), "+v"(cv[1]), "+v"(cv[2]), "+v"(cv[3]), "+v"(cv[4]), "+v"(cv[5]), "+v"(cv[6]), "+v"(cv[7]), "+v"(touch)
                     : : "memory");
        u32x4 sel;
#pragma unroll
        for (int j = 0; j < 4; ++j) sel[j] = (l15 >> 1) == j ? ((l15 & 1) ? 0x3f800000u : 0x00003f80u) : 0u;
        auto gather = [&](auto z_tag) {
            constexpr bool Z = decltype(z_tag)::value;
            f32x4 s[2], z;
            bool first = true;
            for (;;) {
                u32x4 v[8], vz[4];
#pragma unroll
                for (int p = 0; p < 8; ++p) v[p] = ld128(c.rs, src + (4 * (p & 3) + g) * KVS + w * 512 + (p >> 2) * 256 + l15 * 16);
                if constexpr (Z) {
#pragma unroll
                    for (int p = 0; p < 4; ++p) vz[p] = ld128(c.rs, src + (4 * g + p) * KVS + 4096 + l15 * 16);
                }
                if (first) {
                    first = false;
                    f32x4 d = {0.f, 0.f, 0.f, 0.f};
#pragma unroll
                    for (int j = 0; j < 8; ++j) d = mfma16(__builtin_bit_cast(bf16x8, sel), __builtin_bit_cast(bf16x8, cv[j]), d);
                    if (g < 2) *(LAS f32x4*)(lds + CRED + w * 512 + (8 * l15 + 4 * g) * 4) = d;
                }
                s[0] = (f32x4){0.f, 0.f, 0.f, 0.f}; s[1] = s[0]; z = s[0];
#pragma unroll
                for (int p = 0; p < 8; ++p) s[p >> 2] = mfma16(__builtin_bit_cast(bf16x8, sel), __builtin_bit_cast(bf16x8, v[p]), s[p >> 2]);
                if constexpr (Z) {
#pragma unroll
                    for (int p = 0; p < 4; ++p) z += __builtin_bit_cast(f32x4, vz[p]);
                }
                const f32x4 t = (s[0] + s[1]) + z;
                if (!__any(t[0] != t[0] || t[1] != t[1] || t[2] != t[2] || t[3] != t[3])) break;
                unsigned mx = 0;
#pragma unroll
                for (int p = 0; p < 8; ++p) mx = max(mx, umax4(v[p]));
                if constexpr (Z) {
#pragma unroll
                    for (int p = 0; p < 4; ++p) mx = max(mx, umax4(vz[p]));
                }
                if (!__any(mx == SENT) || timed_out(t0)) break;
            }
            if (g < 2) {
                LAS u16* dst = kvt + (w >> 2) * 1056 + (w & 3) * 256 + 8 * l15 + 4 * g;
                *(LAS u32x2*)dst = pack4(s[0]);
                *(LAS u32x2*)(dst + 128) = pack4(s[1]);
            }
            if constexpr (Z) {
#pragma unroll
                for (int j = 0; j < 4; ++j) z[j] = xor16_32_sum(z[j]);
                if (lane < 16) *(LAS u32x2*)(kvt + (lane >> 3) * 1056 + 1024 + (lane & 7) * 4) = (u32x2){pack_bf16(z[0], z[1]), pack_bf16(z[2], z[3])};
            }
        };
        if (w == 1) gather(std::true_type{}); else gather(std::false_type{});
        wait_lgkm0();
        bar();
        STAMP(0, 6);
        LAS u16* kh = kvt + wn * 1056;
        const int e0 = 8 * (l15 >> 2) + (l15 & 3);
        auto rd8 = [&](LAS u16* p) {
            const u32x2 lo = *(LAS u32x2*)(p + 4 * g), hi = *(LAS u32x2*)(p + 16 + 4 * g);
            return __builtin_bit_cast(bf16x8, (u32x4){lo.x, lo.y, hi.x, hi.y});
        };
        const bf16x8 a0 = rd8(kh + e0 * 32), a1 = rd8(kh + (e0 + 4) * 32), az = rd8(kh + 1024);
        const f32x4 zero = {0.f, 0.f, 0.f, 0.f};
#pragma unroll
        for (int mm = 0; mm < 2; ++mm) {
            const f32x4 n0 = mfma16(a0, pq[mm], zero), n1 = mfma16(a1, pq[mm], zero), dn = mfma16(az, pq[mm], zero);
            const float inv = 1.f / (dn[0] + 1e-6f);
            u32x4 r;
            r.x = pack_bf16(n0[0] * inv, n0[1] * inv); r.y = pack_bf16(n0[2] * inv, n0[3] * inv);
            r.z = pack_bf16(n1[0] * inv, n1[1] * inv); r.w = pack_bf16(n1[2] * inv, n1[3] * inv);
            st128(c.rs, WS_O + (hp * 8 + wn * 4 + g) * CHS + (pm * 128 + rbase + 16 * mm + l15) * 16, r);
        }
        if (tid >= 480 && !slow) {
            const int q = tid - 480;
            f32x4 s = {0.f, 0.f, 0.f, 0.f};
#pragma unroll
            for (int j = 0; j < 8; ++j) s += *(LAS f32x4*)(lds + CRED + j * 512 + q * 16);
            st128(c.rs, WS_CS3 + (rr * 7168 + cg * 128 + q * 4) * 4, __builtin_bit_cast(u32x4, s));
        }
    }
    fence();
    if constexpr (MEGA) {
#pragma unroll
        for (int p = 12; p < 16; ++p) gn.pieceB(c, p);
        wait_vm<4>();
    } else {
        wait_vm<0>();
    }
    bar();
    if (tid == 0) st64(c.rs, FL_F3 + pm * 128 + hp * 8, c.tag);
    STAMP(0, 7);
}

template <bool MEGA>
DEVI void phase2(const Ctx& c, const u16* __restrict__ wo, const u16* __restrict__ x,
                 const u16* __restrict__ wg, const u16* __restrict__ wu, const u16* __restrict__ wd) {
    using namespace g2;
    STAMP(2, 0);
    LAS char* lds = c.lds;
    const int tid = c.tid, lane = c.lane, w = c.w;
    const int l15 = lane & 15, g = lane >> 4, q4 = (lane >> 2) & 3, p4 = lane & 3;
    const RsGeom<false> ge(c, wo);
    const int pm = ge.pm, pn = ge.pn, kh = w & 1;

    unsigned voffA[2];
#pragma unroll
    for (int m = 0; m < 2; ++m) voffA[m] = WS_O + (unsigned)((kh * 64 + g) * CHS + (pm * 128 + 16 * (w ^ m) + l15) * 16);
    const int fB = ((q4 >> 1) & 1) | ((g & 1) << 1);
    int vb[4];
#pragma unroll
    for (int n = 0; n < 4; ++n) vb[n] = (8 * g + q4) * 128 + 32 * (n ^ fB) + 8 * p4;
    const int cq = 2 * (g & 1) + (g >> 1), row = pm * 128 + 16 * w + l15;
    const unsigned voffX = (unsigned)(row * DM * 2 + (pn * 8 + cq) * 16);
    if constexpr (!MEGA) {
#pragma unroll
        for (int p = 0; p < 16; ++p) ge.pieceB(c, p);
    }
    fence();
    u32x4 xr[2];
    asm volatile("global_load_dwordx4 %0, %1, %2" : "=v"(xr[0]) : "v"(voffX), "s"(x) : "memory");
    asm volatile("global_load_dwordx4 %0, %1, %2 offset:64" : "=v"(xr[1]) : "v"(voffX), "s"(x) : "memory");
    wait_vm<2>();
    if (w == 0) wait_flags(c.rs, FL_F3 + pm * 128 + l15 * 8, c.tag);
    bar();
    STAMP(2, 1);
    const int rot = ROT2 * (pn & 7);
    u32x4 fa[16][2];
    auto ldA = [&](auto s_tag, auto m_tag) {
        constexpr int s = decltype(s_tag)::value, m = decltype(m_tag)::value;
        if constexpr (s < 16) {
            const int so = __builtin_amdgcn_readfirstlane(((s + rot) & 15) * (4 * CHS));
            asm_load1(c.rs, voffA[m], so, fa[s][m]);
        }
    };
    auto ldA2 = [&](auto s_tag) { if constexpr (decltype(s_tag)::value < DEP2) { ldA(s_tag, IC(0)); ldA(s_tag, IC(1)); } };
    ldA2(IC(0)); ldA2(IC(1)); ldA2(IC(2)); ldA2(IC(3)); ldA2(IC(4)); ldA2(IC(5)); ldA2(IC(6)); ldA2(IC(7));
    ldA2(IC(8)); ldA2(IC(9)); ldA2(IC(10)); ldA2(IC(11));
    bf16x8 fb[3][4];
    auto ldB = [&](int s, int n) {
        int o = B_RING + kh * 8 * B_PIECE + ((s + rot) & 15) * 4096;
        o = o >= LDS_BYTES ? o - LDS_BYTES : o;
        LAS char* pb = lds + vb[n] + o;
        return lds_tr2(pb, pb + 512);
    };
#pragma unroll
    for (int n = 0; n < 4; ++n) { fb[0][n] = ldB(0, n); fb[1][n] = ldB(1, n); }
    f32x4 acc[2][4];
#pragma unroll
    for (int m = 0; m < 2; ++m)
#pragma unroll
        for (int n = 0; n < 4; ++n) acc[m][n] = (f32x4){0.f, 0.f, 0.f, 0.f};
    fence();
    auto step = [&](auto s_tag) {
        constexpr int s = decltype(s_tag)::value;
        constexpr int NY = 2 * ((s + DEP2 < 16 ? s + DEP2 : 16) - s - 1);
        wait_regs2<NY>(fa[s]);
        fence();
#pragma unroll
        for (int i = 0; i < 8; ++i) {
            const int m = i >> 2, n = i & 3;
            acc[m][n] = mfma16(fb[s % 3][n], __builtin_bit_cast(bf16x8, fa[s][m]), acc[m][n]);
            if constexpr (s + 2 < 16) { if (i < 4) fb[(s + 2) % 3][i] = ldB(s + 2, i); }
            if (i == 4) ldA(IC(s + DEP2), IC(0));
            if (i == 6) ldA(IC(s + DEP2), IC(1));
            fence();
        }
    };
    step(IC(0)); step(IC(1)); step(IC(2)); step(IC(3)); step(IC(4)); step(IC(5)); step(IC(6)); step(IC(7));
    step(IC(8)); step(IC(9)); step(IC(10)); step(IC(11)); step(IC(12)); step(IC(13)); step(IC(14)); step(IC(15));
    {
        LAS f32x4* red = (LAS f32x4*)(lds + 32768);
#pragma unroll
        for (int n = 0; n < 4; ++n) red[w * 256 + n * 64 + lane] = acc[1][n];
        wait_lgkm0();
        bar();
#pragma unroll
        for (int n = 0; n < 4; ++n) acc[0][n] += red[(w ^ 1) * 256 + n * 64 + lane];
    }
    STAMP(2, 2);
    asm volatile("" : "+v"(xr[0]), "+v"(xr[1]));

    float hv[16];
#pragma unroll
    for (int i = 0; i < 2; ++i) {
        const u32x4 av = widen(pack4(acc[0][2 * i]), pack4(acc[0][2 * i + 1]));
        u32x4 hb;
#pragma unroll
        for (int j = 0; j < 4; ++j) {
            hb[j] = pack_bf16(bf_lo(xr[i][j]) + bf_lo(av[j]), bf_hi(xr[i][j]) + bf_hi(av[j]));
            hv[8 * i + 2 * j] = bf_lo(hb[j]); hv[8 * i + 2 * j + 1] = bf_hi(hb[j]);
        }
        st128(c.rs, WS_H + (pn * 8 + 4 * i + cq) * CHS + row * 16, hb);
    }
    fence();
    {
        float s = 0.f;
#pragma unroll
        for (int j = 0; j < 16; ++j) s += hv[j];
        const float mu = xor16_32_sum(s) * (1.f / 64.f);
        float m2 = 0.f;
#pragma unroll
        for (int j = 0; j < 16; ++j) { const float d = hv[j] - mu; m2 += d * d; }
        m2 = xor16_32_sum(m2);
        if (lane < 16) st64(c.rs, WS_ST2 + (pn * SEQ + row) * 8, (u32x2){__float_as_uint(mu), __float_as_uint(m2)});
    }
    STAMP(2, 4);
    if (MEGA && w != 0) {
        const G3Geom gn(c, wg, wu, wd);
        gn.prefetchB(c);
        wait_vm<8>();
    } else {
        wait_vm<0>();
    }
    bar();
    if (tid == 0) st64(c.rs, FL_F4 + pm * 128 + pn * 8, c.tag);
    STAMP(2, 7);
}

template <bool MEGA>
DEVI void phase4(const Ctx& c, const u16* __restrict__ wd, u16* __restrict__ out) {
    using namespace g4;
    STAMP(4, 0);
    LAS char* lds = c.lds;
    const int tid = c.tid, lane = c.lane, w = c.w;
    const int wm = w >> 1, wn = w & 1;
    const int l15 = lane & 15, g = lane >> 4, q4 = (lane >> 2) & 3, p4 = lane & 3;
    const RsGeom<true> ge(c, wd);
    const int pm = ge.pm, pn = ge.pn, ks = ge.ks;

    unsigned voffA[2];
#pragma unroll
    for (int j = 0; j < 2; ++j) {
        voffA[j] = (unsigned)((lane >> 3) * CHS + (8 * (2 * w + j) + (lane & 7)) * 16 + (1 - j) * 1024);
    }
    constexpr int ASTEP = 8 * CHS;
    const char* gA = c.ws + WS_HH + (size_t)ks * 28 * ASTEP + pm * 128 * 16 - 1024;
    const unsigned dA = c.lds0 + 2 * w * 1024, dB = c.lds0 + B_RING + 2 * w * 1024;
    int va[2][2], vb[4];
#pragma unroll
    for (int kk = 0; kk < 2; ++kk)
#pragma unroll
        for (int m = 0; m < 2; ++m) va[kk][m] = (4 * wm + 2 * (m ^ ks) + (l15 >> 3)) * 1024 + (4 * kk + g) * 128 + (l15 & 7) * 16;
    {
        const int sB = q4 | ((g & 1) << 2);
#pragma unroll
        for (int n = 0; n < 4; ++n) vb[n] = B_RING + (8 * g + q4) * 256 + 32 * ((4 * wn + n) ^ sB) + 8 * p4;
    }
    if constexpr (!MEGA) {
#pragma unroll
        for (int p = 0; p < 4; ++p) ge.pieceB(c, p);
        wait_vm<0>();
    }
    fence();
    if (w == 0) {
        const int pb = c.bid ^ 1, ppm = (pb & 1) * 4 + ((pb >> 3) & 3), ppn = ((pb & 7) >> 1) * 8 + (pb >> 5);
        wait_flags(c.rs, lane < 16 ? FL_F6 + (pm >> 1) * 256 + ks * 128 + l15 * 8 : FL_F6 + ppm * 256 + ppn * 8, c.tag);
    }
    bar();
    STAMP(4, 1);
#pragma unroll
    for (int p = 0; p < 4; ++p) { glds<0>(gA + p * ASTEP, voffA[0], dA + p * 16384); glds<1024>(gA + p * ASTEP, voffA[1], dA + p * 16384); }

    f32x4 acc[2][4];
#pragma unroll
    for (int m = 0; m < 2; ++m)
#pragma unroll
        for (int n = 0; n < 4; ++n) acc[m][n] = (f32x4){0.f, 0.f, 0.f, 0.f};
    bf16x8 fa[2][2], fb[2][4];
    auto ldA = [&](int s, int kk, int m) { return lds_b128(lds + va[kk][m] + s * 16384); };
    auto ldB = [&](int s, int kk, int n) {
        LAS char* pb = lds + vb[n] + s * B_PIECE + kk * 8192;
        return lds_tr2(pb, pb + 1024);
    };
    u32x4 hr[2];
    const int cq = pn * 16 + wn * 8 + 2 * (g & 1) + (g >> 1), orow = pm * 128 + 32 * wm + 16 * ks + l15;
    const unsigned voffH = WS_H + (unsigned)(cq * CHS + orow * 16);
    const char* pa = gA + 4 * ASTEP;
    const char* pb = ge.gB + (size_t)4 * (64 * 2048);
    auto half = [&](auto s_tag, auto kk_tag, auto iss_tag, auto last_tag) {
        constexpr int s = decltype(s_tag)::value, kk = decltype(kk_tag)::value, ISS = decltype(iss_tag)::value;
        constexpr bool LAST = decltype(last_tag)::value;
        constexpr int ns = kk ? (s + 1) & 3 : s, nk = kk ^ 1;
#pragma unroll
        for (int i = 0; i < 8; ++i) {
            const int m = i >> 2, n = i & 3;
            acc[m][n] = mfma16(fb[kk][n], fa[kk][m], acc[m][n]);
            if constexpr (!LAST) {
                if (i == 0) { fa[nk][0] = ldA(ns, nk, 0); fa[nk][1] = ldA(ns, nk, 1); }
                else if (i <= 4) fb[nk][i - 1] = ldB(ns, nk, i - 1);
            }
            fence();
            if constexpr (kk == 1 && ISS == 1) {
                if (i == 1) glds<0>(pa + s * ASTEP, voffA[0], dA + s * 16384);
                if (i == 3) glds<1024>(pa + s * ASTEP, voffA[1], dA + s * 16384);
                if (i == 5) glds<0>(pb + (size_t)s * (64 * 2048), ge.voffB[0], dB + s * B_PIECE);
                if (i == 7) glds<1024>(pb + (size_t)s * (64 * 2048), ge.voffB[1], dB + s * B_PIECE);
            }
            if constexpr (kk == 1 && ISS == 2) {
                if (i == 1) asm volatile("buffer_load_dwordx4 %0, %1, %2, 0 offen sc1" : "=v"(hr[0]) : "v"(voffH), "s"(c.rs) : "memory");
                if (i == 3) asm volatile("buffer_load_dwordx4 %0, %1, %2, %3 offen sc1" : "=v"(hr[1]) : "v"(voffH), "s"(c.rs), "s"(4 * CHS) : "memory");
            }
            fence();
        }
    };
    auto step = [&](auto s_tag, auto iss_tag, auto wv_tag) {
        constexpr int WV = decltype(wv_tag)::value;
        half(s_tag, IC(0), iss_tag, std::false_type{});
        if constexpr (WV >= 0) {
            wait_vm<WV>();
            wait_lgkm0();
            fence();
            bar();
            fence();
        }
        half(s_tag, IC(1), iss_tag, std::integral_constant<bool, (WV < 0)>{});
    };
    wait_vm<6>();
    bar();
    fa[0][0] = ldA(0, 0, 0); fa[0][1] = ldA(0, 0, 1);
#pragma unroll
    for (int n = 0; n < 4; ++n) fb[0][n] = ldB(0, 0, n);
    fence();
    step(IC(0), IC(1), IC(4)); step(IC(1), IC(1), IC(6)); step(IC(2), IC(1), IC(8)); step(IC(3), IC(1), IC(8));
    for (int it = 0; it < 5; ++it) {
        pa += 4 * ASTEP;
        pb += (size_t)4 * (64 * 2048);
        step(IC(0), IC(1), IC(8)); step(IC(1), IC(1), IC(8)); step(IC(2), IC(1), IC(8)); step(IC(3), IC(1), IC(8));
    }
    step(IC(0), IC(0), IC(8)); step(IC(1), IC(0), IC(4)); step(IC(2), IC(2), IC(0)); step(IC(3), IC(0), IC(-1));
    STAMP(4, 2);

    const unsigned xs = inbox_of(c.bid ^ 1) + (w >> 1) * CHS + (w & 1) * 2048 + lane * 16;
#pragma unroll
    for (int n = 0; n < 2; ++n) {
        u32x4 v = {pack_bf16(acc[1][2 * n][0], acc[1][2 * n][1]), pack_bf16(acc[1][2 * n][2], acc[1][2 * n][3]),
                   pack_bf16(acc[1][2 * n + 1][0], acc[1][2 * n + 1][1]), pack_bf16(acc[1][2 * n + 1][2], acc[1][2 * n + 1][3])};
        st128(c.rs, xs + n * 1024, no_sent(__builtin_bit_cast(f32x4, v)));
    }
    const unsigned xr = inbox_of(c.bid) + (w >> 1) * CHS + (w & 1) * 2048 + lane * 16;
    u32x4 pv[2];
    {
        const u64 t0 = __builtin_amdgcn_s_memrealtime();
        for (;;) {
#pragma unroll
            for (int n = 0; n < 2; ++n) pv[n] = ld128(c.rs, xr + n * 1024);
            const unsigned mx = max(umax4(pv[0]), umax4(pv[1]));
            if (!__any(mx == SENT) || timed_out(t0)) break;
            __builtin_amdgcn_s_sleep(1);
        }
    }
    STAMP(4, 3);
    asm volatile("" : "+v"(hr[0]), "+v"(hr[1]));
#pragma unroll
    for (int i = 0; i < 2; ++i) {
        u32x2 d[2];
#pragma unroll
        for (int k = 0; k < 2; ++k) {
            const unsigned p0 = pv[i][2 * k], p1 = pv[i][2 * k + 1];
            const f32x4 a = acc[0][2 * i + k];
            d[k] = (u32x2){pack_bf16(a[0] + bf_lo(p0), a[1] + bf_hi(p0)), pack_bf16(a[2] + bf_lo(p1), a[3] + bf_hi(p1))};
        }
        const u32x4 av = widen(d[0], d[1]);
        u32x4 o;
#pragma unroll
        for (int j = 0; j < 4; ++j) o[j] = pack_bf16(bf_lo(hr[i][j]) + bf_lo(av[j]), bf_hi(hr[i][j]) + bf_hi(av[j]));
        *(u32x4*)(out + (size_t)orow * DM + (cq + 4 * i) * 8) = o;
    }
    STAMP(4, 7);
}

template <bool MEGA>
DEVI void phase3(const Ctx& c, const u16* __restrict__ wg, const u16* __restrict__ wu, const u16* __restrict__ wd) {
    using namespace g3;
    STAMP(3, 0);
    LAS char* lds = c.lds;
    const int tid = c.tid, lane = c.lane, w = c.w;
    const int grp = w >> 2, wm = w >> 1, wn = w & 1;
    const int l15 = lane & 15, g = lane >> 4, q4 = (lane >> 2) & 3, p4 = lane & 3;
    const G3Geom ge(c, wg, wu, wd);
    const int pm = ge.pm, pn = ge.pn;

    unsigned voffA[2];
#pragma unroll
    for (int j = 0; j < 2; ++j) {
        voffA[j] = (unsigned)((lane >> 3) * CHS + (8 * (2 * w + j) + (lane & 7)) * 16 + (1 - j) * 1024);
    }
    const char* gA = c.ws + WS_H + pm * 256 * 16 - 1024;
    const unsigned ldsA = c.lds0 + 2 * w * 1024;

    if (w == 0) {
        const int cgp = (pn * 112 + (lane & 1) * 111) / 128 + ((lane >> 1) & 1) * 28, pb = ((lane >> 2) & 3) * 56 + cgp;
        const int ppm = pb & 15, hi = pb >> 4, php = hi + (hi >= c.bad) + (hi >= c.bad + 7);
        wait_flags(c.rs, lane < 32 ? FL_F4 + pm * 256 + lane * 8 : FL_F3 + ppm * 128 + php * 8, c.tag);
    }
    if (w == 0 || !MEGA) ge.prefetchB(c);
    bar();
    STAMP(3, 1);
    auto issueA = [&](int p, int slot) {
        const char* a = gA + (size_t)(p & 1) * AHALF + (p >> 1) * ASTEP;
        glds<0>(a, voffA[0], ldsA + slot * A_PIECE); glds<1024>(a, voffA[1], ldsA + slot * A_PIECE);
    };
    issueA(0, 0); issueA(1, 1);
    f32x4 sv[4];
    float cp[4];
    auto load_stats = [&]() {
        asm_load4<SEQ * 8>(c.rs, WS_ST2 + ((tid & 3) * 4 * SEQ + pm * 256) * 8 + (tid >> 2) * 16, sv);
        const int t2 = min(tid, 223);
        const unsigned o = WS_CS3 + (unsigned)(pn * 112 + t2 + (t2 >= 112 ? DF - 112 : 0)) * 4;
        asm volatile("buffer_load_dword %0, %4, %5, 0 offen sc1\n\tbuffer_load_dword %1, %4, %5, %6 offen sc1\n\t"
                     "buffer_load_dword %2, %4, %5, %7 offen sc1\n\tbuffer_load_dword %3, %4, %5, %8 offen sc1"
                     : "=&v"(cp[0]), "=&v"(cp[1]), "=&v"(cp[2]), "=&v"(cp[3])
                     : "v"(o), "s"(c.rs), "s"(7168 * 4), "s"(2 * 7168 * 4), "s"(3 * 7168 * 4)
                     : "memory");
    };
    issueA(2, 2);
    if (grp == 1) issueA(3, 3);
    LAS f32x4* rstat = (LAS f32x4*)(lds + TAB_OFF);
    LAS float* ctab = (LAS float*)(lds + TAB_OFF + 4096);
    auto row_table = [&]() {
        float mean[2], m2[2];
#pragma unroll
        for (int r = 0; r < 2; ++r) {
            mean[r] = 0.25f * ((sv[0][2 * r] + sv[1][2 * r]) + (sv[2][2 * r] + sv[3][2 * r]));
            float dd = 0.f, q = 0.f;
#pragma unroll
            for (int i = 0; i < 4; ++i) { const float d = sv[i][2 * r] - mean[r]; dd += d * d; q += sv[i][2 * r + 1]; }
            m2[r] = q + 64.f * dd;
        }
        const bool odd = tid & 1;
        const float mo = __shfl_xor(odd ? mean[0] : mean[1], 1), qo = __shfl_xor(odd ? m2[0] : m2[1], 1);
        const float mk = odd ? mean[1] : mean[0], qk = odd ? m2[1] : m2[0];
        const float d1 = mk - mo, ma = 0.5f * (mk + mo), qa = qk + qo + 128.f * d1 * d1;
        const float mb = __shfl_xor(ma, 2), qb = __shfl_xor(qa, 2);
        const float d = ma - mb;
        const float ve = (qa + qb + 256.f * d * d) * (1.f / DM) + LN_EPS;
        const float r = rsqrtf(ve);
        const float c1 = 2.3022081985f, c2 = 0.1029432397f;
        if ((tid & 2) == 0) rstat[2 * (tid >> 2) + (tid & 1)] = (f32x4){0.5f * (ma + mb), -c1 * r, -c2 * r * r * r, ve};
        asm volatile("" : "+v"(cp[0]), "+v"(cp[1]), "+v"(cp[2]), "+v"(cp[3]));
        if (tid < 224) ctab[tid] = (cp[0] + cp[1]) + (cp[2] + cp[3]);
    };

    int aoff[2];
#pragma unroll
    for (int ks = 0; ks < 2; ++ks) aoff[ks] = (8 * (wm & 1) + (l15 >> 3)) * 1024 + (4 * ks + g) * 128 + (l15 & 7) * 16;
    const int brow = (16 * (g >> 1) + 4 * (g & 1) + q4) * 224;
    const int boff = B_OFF + brow + 112 * wn + 8 * p4;
    const int boffX = B_OFF + brow + 112 * wn + 96 + (p4 < 2 ? 8 * p4 : B_SUB + 8 * (p4 - 2));

    f32x4 acc[4][7];
#pragma unroll
    for (int m = 0; m < 4; ++m)
#pragma unroll
        for (int n = 0; n < 7; ++n) acc[m][n] = (f32x4){0.f, 0.f, 0.f, 0.f};

    unsigned voffAe[2], voffAo[2], voffBo[2];
#pragma unroll
    for (int j = 0; j < 2; ++j) {
        voffAe[j] = voffA[j] + (grp ? 2 * ASTEP : AHALF + ASTEP);
        voffAo[j] = voffA[j] + (grp ? AHALF + 2 * ASTEP : 2 * ASTEP);
        voffBo[j] = ge.voffB[j] + BSTEP;
    }
    const char* pA = gA;
    const char* pB0 = ge.gB[0] + (size_t)4 * BSTEP;
    const char* pB1 = ge.gB[1] + (size_t)4 * BSTEP;
    int rdB = 0, prevB = 4 * B_PIECE;
    int rdA = grp * A_PIECE;
    int vB = boff, vBX = boffX, vA0 = aoff[0] + rdA, vA1 = aoff[1] + rdA;
    auto ringA = [&](int xx, int k) { xx += k * A_PIECE; return xx >= NR * A_PIECE ? xx - NR * A_PIECE : xx; };

    auto unit = [&](auto ks_tag, auto iss_tag, auto wv_tag) {
        constexpr int ks = decltype(ks_tag)::value, ISS = decltype(iss_tag)::value, WV = decltype(wv_tag)::value;
        bf16x8 a[4], b[7];
#pragma unroll
        for (int n = 0; n < 6; ++n) {
            LAS char* pb = lds + vB + (n >= 3 ? B_SUB : 0) + (n % 3) * 32;
            b[n] = lds_tr2(pb, pb + 8 * 224);
        }
        { LAS char* pb = lds + vBX; b[6] = lds_tr2(pb, pb + 8 * 224); }
#pragma unroll
        for (int m = 0; m < 4; ++m) a[m] = lds_b128(lds + (ks ? vA1 : vA0) + m * 2048);
        fence();
        const unsigned dA = ldsA + ringA(rdA, 3 + ks), dB = ge.ldsB + ((unsigned)prevB & ge.ringmask);
        const char* qA = pA; const char* qB0 = pB0; const char* qB1 = pB1;
        if constexpr (ISS == 1) {
            if constexpr (ks == 1) { pA += ASTEP; pB0 += 2 * BSTEP; pB1 += 2 * BSTEP; }
        } else if constexpr (ISS == 2) {
            if (grp == 0) { glds<0>(qA, voffAe[0], dA); glds<1024>(qA, voffAe[1], dA); }
        }
        prevB = rdB;
        rdB = rdB == (NR - 1) * B_PIECE ? 0 : rdB + B_PIECE;
        vB = boff + rdB; vBX = boffX + rdB;
        if constexpr (ks == 1) { rdA = ringA(rdA, 2); vA0 = aoff[0] + rdA; vA1 = aoff[1] + rdA; }
        fence();
        if constexpr (WV >= 0) wait_vm<WV>();
        wait_lgkm0();
        fence();
        bar();
        fence();
        __builtin_amdgcn_s_setprio(1);
#pragma unroll
        for (int m = 0; m < 4; ++m) {
#pragma unroll
            for (int n = 0; n < 7; ++n) {
                acc[m][n] = mfma16(b[n], a[m], acc[m][n]);
                if constexpr (ISS == 1) {
                    if (n == 3) {
                        fence();
                        if (m == 0) glds<0>(qA, ks ? voffAo[0] : voffAe[0], dA);
                        if (m == 1) glds<1024>(qA, ks ? voffAo[1] : voffAe[1], dA);
                        if (m == 2) glds<0>(qB0, ks ? voffBo[0] : ge.voffB[0], dB);
                        if (m == 3) glds<1024>(qB1, ks ? voffBo[1] : ge.voffB[1], dB);
                        fence();
                    }
                }
                if constexpr (ISS == 2 && P3LATE) {
                    if (n == 3 && m == 0) { fence(); load_stats(); fence(); }
                }
                if constexpr (ISS == 3) {
                    if (n == 3 && m < 2) {
                        fence();
                        st128(c.rs, inbox_of(c.bid) + (2 * m + (tid >> 8)) * CHS + (tid & 255) * 16, (u32x4){SENT, SENT, SENT, SENT});
                        fence();
                    }
                }
            }
        }
        __builtin_amdgcn_s_setprio(0);
        fence();
        if (WV >= 0 || grp == 0) bar();
        fence();
    };
    wait_vm<4>();
    bar();
    if (grp == 1) bar();
    unit(IC(0), IC(1), IC(2));
    unit(IC(1), IC(1), IC(4));
    for (int t = 1; t < 14; ++t) {
        unit(IC(0), IC(1), IC(6));
        unit(IC(1), IC(1), IC(6));
    }
    unit(IC(0), IC(2), IC(8));
    unit(IC(1), IC(0), IC(10));
    unit(IC(0), IC(3), IC(0));
    wait_regs4<2>(sv);
    row_table();
    unit(IC(1), IC(0), IC(-1));
    STAMP(3, 2);
    if constexpr (MEGA) {
        const RsGeom<true> gn(c, wd);
#pragma unroll
        for (int p = 0; p < 4; ++p) gn.pieceB(c, p);
    }
    f32x4 cw[7];
#pragma unroll
    for (int n = 0; n < 6; ++n) cw[n] = *(LAS f32x4*)(ctab + (n / 3) * 112 + wn * 56 + (n % 3) * 16 + 4 * g);
    cw[6] = *(LAS f32x4*)(ctab + (g >> 1) * 112 + wn * 56 + 48 + 4 * (g & 1));
    f32x4 st[4];
#pragma unroll
    for (int m = 0; m < 4; ++m) st[m] = rstat[64 * wm + 16 * m + l15];
    auto geglu2 = [](f32x2 G, f32x2 U, const f32x4& s) {
        const f32x2 a = G * (G * G * s[2] + s[1]);
        f32x2 e; e.x = __builtin_amdgcn_exp2f(a.x); e.y = __builtin_amdgcn_exp2f(a.y);
        const f32x2 d = e * s[3] + s[3];
        f32x2 r; r.x = __builtin_amdgcn_rcpf(d.x); r.y = __builtin_amdgcn_rcpf(d.y);
        const f32x2 o = (G * U) * r;
        return pack_bf16(o.x, o.y);
    };
    const unsigned gdst = WS_HH + (unsigned)((pn * 14 + wn * 7 + (g >> 1)) * CHS + (pm * 256 + 64 * wm + 16 * (g & 1) + l15) * 16);
    u32x2 zz[2];
#pragma unroll
    for (int mp = 0; mp < 2; ++mp) {
#pragma unroll
        for (int n = 0; n < 3; ++n) {
            u32x2 o[2];
#pragma unroll
            for (int i = 0; i < 2; ++i) {
                const int m = 2 * mp + i;
                const f32x4 gg = acc[m][n] - st[m][0] * cw[n];
                const f32x4 uu = acc[m][n + 3] - st[m][0] * cw[n + 3];
                o[i].x = geglu2(gg.xy, uu.xy, st[m]);
                o[i].y = geglu2(gg.zw, uu.zw, st[m]);
            }
            st128(c.rs, gdst + n * (2 * CHS) + mp * 512, widen(o[0], o[1]));
            fence();
        }
        const f32x4 y0 = acc[2 * mp][6] - st[2 * mp][0] * cw[6];
        const f32x4 y1 = acc[2 * mp + 1][6] - st[2 * mp + 1][0] * cw[6];
        const f32x4 sx = lane < 32 ? st[2 * mp] : st[2 * mp + 1];
        f32x4 gg, uu;
#pragma unroll
        for (int j = 0; j < 4; ++j) {
            auto r = __builtin_amdgcn_permlane32_swap(__float_as_uint(y0[j]), __float_as_uint(y1[j]), false, false);
            gg[j] = __uint_as_float(r[0]); uu[j] = __uint_as_float(r[1]);
        }
        zz[mp] = (u32x2){geglu2(gg.xy, uu.xy, sx), geglu2(gg.zw, uu.zw, sx)};
        STAMP(3, 3 + mp);
    }
    st128(c.rs, WS_HH + (unsigned)((pn * 14 + wn * 7 + 6) * CHS + (pm * 256 + 64 * wm + 16 * (2 * (g & 1) + (g >> 1)) + l15) * 16), widen(zz[0], zz[1]));
    STAMP(3, 5);
    wait_vm<0>();
    bar();
    if (tid == 0) st64(c.rs, FL_F6 + pm * 256 + pn * 8, c.tag);
    STAMP(3, 7);
}

template <int PHASES>
__global__ __launch_bounds__(512, 2) void fused_kernel(const u16* __restrict__ x, const u16* __restrict__ wqkv,
                                                       const u16* __restrict__ wo, const u16* __restrict__ wg,
                                                       const u16* __restrict__ wu, const u16* __restrict__ wd,
                                                       u16* __restrict__ out, char* __restrict__ ws) {
    constexpr bool MEGA = PHASES == 15;
    extern __shared__ __attribute__((aligned(16))) char smem_raw[];
    Ctx c;
    c.lds = (LAS char*)smem_raw;
    c.lds0 = (unsigned)(uintptr_t)c.lds;
    c.rs = __builtin_amdgcn_make_buffer_rsrc((void*)ws, 0, WS_END, 0x00020000);
    c.ws = ws;
    c.tid = threadIdx.x; c.lane = c.tid & 63;
    c.w = __builtin_amdgcn_readfirstlane(c.tid >> 6);
    c.bid = blockIdx.x;
    c.bad = (3 - (int)((uintptr_t)wqkv >> 7)) & 7;
    {
        asm volatile("buffer_load_dwordx2 %0, off, %1, 0 sc1" : "=v"(c.tag) : "s"(c.rs) : "memory");
        if constexpr (!(PHASES & 1)) finish_tag<0>(c);
    }
    if constexpr (PHASES & 1) phase1<MEGA>(c, x, wqkv, wo, wg, wu);
    fresh_ids(c);
    if constexpr (PHASES & 2) phase2<MEGA>(c, wo, x, wg, wu, wd);
    fresh_ids(c);
    if constexpr (PHASES & 4) phase3<MEGA>(c, wg, wu, wd);
    fresh_ids(c);
    if constexpr (PHASES & 8) {
        phase4<MEGA>(c, wd, out);
        if (c.bid == 0 && c.tid == 0) st64(c.rs, FL_EPOCH, c.tag);
    }
}

template <int PHASES>
static void launch_phases(void* const* d_in, void* d_out, void* d_ws, hipStream_t stream) {
    (void)hipFuncSetAttribute((const void*)fused_kernel<PHASES>, hipFuncAttributeMaxDynamicSharedMemorySize, LDS_BYTES);
    fused_kernel<PHASES><<<256, 512, LDS_BYTES, stream>>>((const u16*)d_in[0], (const u16*)d_in[1], (const u16*)d_in[2],
                                                          (const u16*)d_in[4], (const u16*)d_in[5], (const u16*)d_in[6],
                                                          (u16*)d_out, (char*)d_ws);
}

extern "C" void kernel_launch(void* const* d_in, const int* in_sizes, int n_in, void* d_out, int out_size,
                              void* d_ws, size_t ws_size, hipStream_t stream) {
    launch_phases<15>(d_in, d_out, d_ws, stream);
}
```
